```python
import torch

def _softmax(x, axis=-1):
    return torch.softmax(x, dim=axis)
def _gelu(x):
    return torch.nn.functional.gelu(x, approximate="tanh")
def _silu(x):
    return torch.nn.functional.silu(x)
def _sigmoid(x):
    return torch.sigmoid(x)
def _layernorm(x, axis=-1, eps=1e-5):
    m = x.mean(dim=axis, keepdim=True)
    v = x.var(dim=axis, keepdim=True, unbiased=False)
    return (x - m) / torch.sqrt(v + eps)
def _scatter_add(tgt, idx, src, axis=0):
    return tgt.clone().index_add_(axis, idx, src)
def _segment_sum(src, seg, n):
    out = torch.zeros((n,) + src.shape[1:], dtype=src.dtype)
    return out.index_add_(0, seg, src)
def _pad(x, pw):
    flat = tuple(v for pair in reversed(pw) for v in pair)
    return torch.nn.functional.pad(x, flat)

kernel_name = 'standalone__layernorm_linear__sampling'

def setup_inputs():

    _w = (lambda a: (lambda t: t.to(torch.bfloat16) if t.is_floating_point() else t)(torch.as_tensor(a)))
    B, S, D = 1024, 32, 448
    return {
        "x": _w((lambda *s: torch.randn(*s, dtype=torch.bfloat16) * 0.05)(B, S, D)),
        "g": _w((lambda *s: torch.randn(*s, dtype=torch.bfloat16) * 0.05)(D) + 1.0),
        "b": _w((lambda *s: torch.randn(*s, dtype=torch.bfloat16) * 0.4)(D)),
        "w": _w((lambda *s: torch.randn(*s, dtype=torch.bfloat16) * 0.05)(D, D)),
    }

def reference(x, g, b, w):
    return (_layernorm(x, axis=-1) * g + b) @ w
```

```cpp
#include <hip/hip_runtime.h>
#include <hip/hip_bf16.h>
typedef __attribute__((ext_vector_type(8))) short short8;
typedef __attribute__((ext_vector_type(8))) __bf16 bf16x8;
typedef __attribute__((ext_vector_type(4))) float f32x4;
#define D 448
__device__ __forceinline__ float bf2f(unsigned short u){ return __uint_as_float(((unsigned)u)<<16); }
__device__ __forceinline__ unsigned short f2bf(float f){ __bf16 b=(__bf16)f; return __builtin_bit_cast(unsigned short,b); }
__global__ __launch_bounds__(256) void k(const unsigned short* __restrict__ x,const unsigned short* __restrict__ g,const unsigned short* __restrict__ b,const unsigned short* __restrict__ w,unsigned short* __restrict__ out){
  __shared__ __attribute__((aligned(16))) unsigned short xs[64*(D+8)];
  int tid=threadIdx.x, lane=tid&63, wv=tid>>6;
  int row0=blockIdx.x*64;
  for(int r=wv;r<64;r+=4){
    const unsigned short* xr=x+(size_t)(row0+r)*D;
    float v[7]; float s=0;
    for(int i=0;i<7;i++){ v[i]=bf2f(xr[i*64+lane]); s+=v[i]; }
    for(int o=32;o;o>>=1) s+=__shfl_xor(s,o);
    float m=s*(1.0f/D); float q=0;
    for(int i=0;i<7;i++){ v[i]-=m; q+=v[i]*v[i]; }
    for(int o=32;o;o>>=1) q+=__shfl_xor(q,o);
    float rs=rsqrtf(q*(1.0f/D)+1e-5f);
    for(int i=0;i<7;i++){ int c=i*64+lane; xs[r*(D+8)+c]=f2bf(v[i]*rs*bf2f(g[c])+bf2f(b[c])); }
  }
  __syncthreads();
  f32x4 acc[4][7];
  for(int i=0;i<4;i++)for(int j=0;j<7;j++)acc[i][j]=(f32x4){0,0,0,0};
  int n0=wv*112;
  for(int ks=0;ks<14;ks++){
    int kb=ks*32+8*(lane>>4);
    short8 a[4];
    for(int i=0;i<4;i++) a[i]=*(const short8*)&xs[(i*16+(lane&15))*(D+8)+kb];
    for(int j=0;j<7;j++){
      short8 bb;
      for(int e=0;e<8;e++) bb[e]=w[(size_t)(kb+e)*D+n0+j*16+(lane&15)];
      for(int i=0;i<4;i++) acc[i][j]=__builtin_amdgcn_mfma_f32_16x16x32_bf16(__builtin_bit_cast(bf16x8,a[i]),__builtin_bit_cast(bf16x8,bb),acc[i][j],0,0,0);
    }
  }
  for(int i=0;i<4;i++)for(int j=0;j<7;j++)for(int r=0;r<4;r++)
    out[(size_t)(row0+i*16+(lane>>4)*4+r)*D+n0+j*16+(lane&15)]=f2bf(acc[i][j][r]);
}
extern "C" void kernel_launch(void* const* d_in,const int* in_sizes,int n_in,void* d_out,int out_size,void* d_ws,size_t ws_size,hipStream_t stream){
  int M=in_sizes[0]/D;
  k<<<M/64,256,0,stream>>>((const unsigned short*)d_in[0],(const unsigned short*)d_in[1],(const unsigned short*)d_in[2],(const unsigned short*)d_in[3],(unsigned short*)d_out);
}
```

```cpp
#include <hip/hip_runtime.h>

__global__ __launch_bounds__(512, 2) void lnlin_kernel(const char* x, const char* g, const char* b, const char* wt, char* out) {
  __shared__ __attribute__((aligned(1024))) char smem[163840];
  const int wv = __builtin_amdgcn_readfirstlane(threadIdx.x >> 6);
  const int bid = blockIdx.x;
  const char* xp = x + (size_t)bid * 114688;
  char* op = out + (size_t)bid * 114688;
  asm volatile(
      ".Lcode_begin:\n"
      "v_mbcnt_lo_u32_b32 v224, -1, 0\n"
      "v_mbcnt_hi_u32_b32 v224, -1, v224\n"
      "v_lshlrev_b32 v227, 4, v224\n"
      "s_mul_i32 s64, %[w], 7168\n"
      "s_cmp_eq_u32 %[role], 0\n"
      "s_cselect_b64 s[52:53], %[gp], %[bp]\n"
      "s_and_b32 s53, s53, 0xffff\n"
      "s_movk_i32 s54, 896\n"
      "s_mov_b32 s55, 0x20000\n"
      "s_lshl_b32 s71, %[role], 10\n"
      "s_add_u32 m0, s71, 149504\n"
      "s_nop 0\n"
      "buffer_load_dwordx4 v227, s[52:55], 0 offen lds\n"
      "s_cmp_eq_u32 %[role], 0\n"
      "s_cbranch_scc1 .Lrole_n\n"
      "s_mov_b64 s[40:41], %[xp]\n"
      "s_and_b32 s41, s41, 0xffff\n"
      "s_mov_b32 s42, 114688\n"
      "s_mov_b32 s43, 0x20000\n"
      "s_mov_b32 m0, s64\n"
      "s_nop 0\n"
      "buffer_load_dwordx4 v227, s[40:43], m0 offen offset:0 nt lds\n"
      "buffer_load_dwordx4 v227, s[40:43], m0 offen offset:1024 nt lds\n"
      "buffer_load_dwordx4 v227, s[40:43], m0 offen offset:2048 nt lds\n"
      "buffer_load_dwordx4 v227, s[40:43], m0 offen offset:3072 nt lds\n"
      "s_mov_b64 s[44:45], %[wp]\n"
      "s_and_b32 s45, s45, 0xffff\n"
      "s_mov_b32 s46, 401408\n"
      "s_mov_b32 s47, 0x20000\n"
      "s_mul_i32 s39, %[sl], 12544\n"
      "s_getpc_b64 s[60:61]\n"
      ".Lpc_here1:\n"
      "s_sub_u32 s60, s60, .Lpc_here1-.Lcode_begin\n"
      "s_subb_u32 s61, s61, 0\n"
      "s_and_b32 s61, s61, 0xffff\n"
      "s_mov_b32 s62, .Lcode_end-.Lcode_begin\n"
      "s_mov_b32 s63, 0x20000\n"
      "s_lshl_b32 s71, %[sl], 3\n"
      "s_add_u32 s71, s71, %[wv]\n"
      "s_lshl_b32 s71, s71, 7\n"
      "v_lshlrev_b32 v225, 15, v224\n"
      "s_mov_b64 exec, 3\n"
      "buffer_load_dword v231, v225, s[60:63], s71 offen\n"
      "s_mov_b64 exec, -1\n"
      "s_addk_i32 m0, 4096\n"
      "s_nop 0\n"
      "buffer_load_dwordx4 v227, s[40:43], m0 offen offset:0 nt lds\n"
      "buffer_load_dwordx4 v227, s[40:43], m0 offen offset:1024 nt lds\n"
      "buffer_load_dwordx4 v227, s[40:43], m0 offen offset:2048 nt lds\n"
      "s_setprio 0\n"
      "s_mul_i32 s71, %[w], 3584\n"
      "s_add_u32 s39, s71, s39\n"
      "v_mul_u32_u24 v230, 896, v224\n"
      "v_lshrrev_b32 v225, 5, v224\n"
      "v_bfe_u32 v226, v224, 3, 1\n"
      "v_lshl_add_u32 v229, v225, 4, v226\n"
      "v_bfe_u32 v226, v224, 4, 1\n"
      "v_lshl_add_u32 v229, v226, 2, v229\n"
      "v_mul_u32_u24 v229, 896, v229\n"
      "v_bfe_u32 v225, v224, 1, 2\n"
      "v_bfe_u32 v226, v224, 4, 2\n"
      "v_xor_b32 v225, v225, v226\n"
      "v_lshl_add_u32 v229, v225, 5, v229\n"
      "v_and_b32 v225, 1, v224\n"
      "v_lshl_add_u32 v229, v225, 4, v229\n"
      "s_lshr_b32 s71, %[w], 1\n"
      "s_lshl_b32 s71, s71, 3\n"
      "s_and_b32 s67, %[w], 1\n"
      "s_lshl_b32 s67, s67, 5\n"
      "s_add_u32 s71, s71, s67\n"
      "s_mul_i32 s71, s71, 896\n"
      "v_add_u32 v229, s71, v229\n"
      "s_lshl_b32 s66, %[w], 10\n"
      "v_bfe_u32 v225, v224, 2, 2\n"
      "v_mul_u32_u24 v228, 7168, v225\n"
      "v_and_b32 v225, 2, v225\n"
      "v_lshrrev_b32 v226, 4, v224\n"
      "v_xor_b32 v225, v225, v226\n"
      "v_lshl_add_u32 v228, v225, 4, v228\n"
      "v_and_b32 v226, 3, v224\n"
      "v_lshl_add_u32 v228, v226, 6, v228\n"
      "s_lshr_b32 s71, %[w], 1\n"
      "s_mul_i32 s71, s71, 57344\n"
      "v_add_u32 v228, s71, v228\n"
      "s_and_b32 s71, %[w], 1\n"
      "v_bfe_u32 v225, v224, 3, 2\n"
      "v_xor_b32 v225, s71, v225\n"
      "v_lshrrev_b32 v226, 2, v224\n"
      "v_lshlrev_b32 v252, 7, v226\n"
      "v_lshl_add_u32 v252, v225, 5, v252\n"
      "v_and_b32 v226, 3, v224\n"
      "v_lshl_add_u32 v252, v226, 3, v252\n"
      "v_xor_b32 v253, 64, v252\n"
      "v_add_u32 v254, 92160, v252\n"
      "v_add_u32 v255, 92160, v253\n"
      "s_lshr_b32 s38, %[w], 1\n"
      "s_mul_i32 s38, s38, 8704\n"
      "s_and_b32 s71, %[w], 1\n"
      "s_mul_i32 s71, s71, 528\n"
      "s_add_u32 s38, s38, s71\n"
      "s_waitcnt vmcnt(8)\n"
      "s_barrier\n"
      "s_addk_i32 m0, 24576\n"
      "s_waitcnt vmcnt(7)\n"
      "buffer_load_dwordx4 v227, s[40:43], m0 offen offset:0 nt lds\n"
      "s_waitcnt vmcnt(7)\n"
      "buffer_load_dwordx4 v227, s[40:43], m0 offen offset:1024 nt lds\n"
      "s_waitcnt vmcnt(7)\n"
      "buffer_load_dwordx4 v227, s[40:43], m0 offen offset:2048 nt lds\n"
      "s_waitcnt vmcnt(7)\n"
      "s_barrier\n"
      "buffer_load_dwordx4 v227, s[40:43], m0 offen offset:3072 nt lds\n"
      "s_addk_i32 m0, 4096\n"
      "s_waitcnt vmcnt(6)\n"
      "buffer_load_dwordx4 v227, s[40:43], m0 offen offset:0 nt lds\n"
      "s_waitcnt vmcnt(6)\n"
      "buffer_load_dwordx4 v227, s[40:43], m0 offen offset:1024 nt lds\n"
      "s_mov_b64 exec, 15\n"
      "buffer_load_dword v231, v230, s[44:47], s39 offen offset:0\n"
      "s_mov_b64 exec, -1\n"
      "s_waitcnt vmcnt(7)\n"
      "s_barrier\n"
      "s_lshr_b32 s71, %[w], 1\n"
      "s_cmp_lg_u32 s71, 0\n"
      "s_cbranch_scc1 .Lfr0\n"
      "ds_read_b128 v[0:3], v228 offset:0\n"
      "ds_read_b128 v[4:7], v228 offset:256\n"
      "ds_read_b128 v[8:11], v228 offset:512\n"
      "ds_read_b128 v[12:15], v228 offset:768\n"
      "ds_read_b128 v[16:19], v228 offset:1024\n"
      "ds_read_b128 v[20:23], v228 offset:1280\n"
      "ds_read_b128 v[24:27], v228 offset:1536\n"
      "ds_read_b128 v[28:31], v228 offset:1792\n"
      "ds_read_b128 v[32:35], v228 offset:2048\n"
      "ds_read_b128 v[36:39], v228 offset:2304\n"
      "ds_read_b128 v[40:43], v228 offset:2560\n"
      "ds_read_b128 v[44:47], v228 offset:2816\n"
      "ds_read_b128 v[48:51], v228 offset:3072\n"
      "ds_read_b128 v[52:55], v228 offset:3328\n"
      ".Lfr0:\n"
      "buffer_load_dwordx4 v227, s[40:43], m0 offen offset:2048 nt lds\n"
      "s_addk_i32 m0, 24576\n"
      "s_waitcnt vmcnt(7)\n"
      "buffer_load_dwordx4 v227, s[40:43], m0 offen offset:0 nt lds\n"
      "s_waitcnt vmcnt(7)\n"
      "buffer_load_dwordx4 v227, s[40:43], m0 offen offset:1024 nt lds\n"
      "s_waitcnt vmcnt(7)\n"
      "buffer_load_dwordx4 v227, s[40:43], m0 offen offset:2048 nt lds\n"
      "s_waitcnt vmcnt(7) lgkmcnt(0)\n"
      "s_barrier\n"
      "s_lshr_b32 s71, %[w], 1\n"
      "s_cmp_lg_u32 s71, 0\n"
      "s_cbranch_scc1 .Lfr1\n"
      "ds_read_b128 v[56:59], v228 offset:3584\n"
      "ds_read_b128 v[60:63], v228 offset:3840\n"
      "ds_read_b128 v[64:67], v228 offset:4096\n"
      "ds_read_b128 v[68:71], v228 offset:4352\n"
      "ds_read_b128 v[72:75], v228 offset:4608\n"
      "ds_read_b128 v[76:79], v228 offset:4864\n"
      "ds_read_b128 v[80:83], v228 offset:5120\n"
      "ds_read_b128 v[84:87], v228 offset:5376\n"
      "ds_read_b128 v[88:91], v228 offset:5632\n"
      "ds_read_b128 v[92:95], v228 offset:5888\n"
      "ds_read_b128 v[96:99], v228 offset:6144\n"
      "ds_read_b128 v[100:103], v228 offset:6400\n"
      "ds_read_b128 v[104:107], v228 offset:6656\n"
      "ds_read_b128 v[108:111], v228 offset:6912\n"
      ".Lfr1:\n"
      "buffer_load_dwordx4 v227, s[40:43], m0 offen offset:3072 nt lds\n"
      "s_addk_i32 m0, 4096\n"
      "s_waitcnt vmcnt(7)\n"
      "buffer_load_dwordx4 v227, s[40:43], m0 offen offset:0 nt lds\n"
      "s_waitcnt vmcnt(7)\n"
      "buffer_load_dwordx4 v227, s[40:43], m0 offen offset:1024 nt lds\n"
      "s_waitcnt vmcnt(6) lgkmcnt(0)\n"
      "s_barrier\n"
      "s_lshr_b32 s71, %[w], 1\n"
      "s_cmp_lg_u32 s71, 0\n"
      "s_cbranch_scc1 .Lfr2\n"
      "ds_read_b128 v[112:115], v228 offset:28672\n"
      "ds_read_b128 v[116:119], v228 offset:28928\n"
      "ds_read_b128 v[120:123], v228 offset:29184\n"
      "ds_read_b128 v[124:127], v228 offset:29440\n"
      "ds_read_b128 v[128:131], v228 offset:29696\n"
      "ds_read_b128 v[132:135], v228 offset:29952\n"
      "ds_read_b128 v[136:139], v228 offset:30208\n"
      "ds_read_b128 v[140:143], v228 offset:30464\n"
      "ds_read_b128 v[144:147], v228 offset:30720\n"
      "ds_read_b128 v[148:151], v228 offset:30976\n"
      "ds_read_b128 v[152:155], v228 offset:31232\n"
      "ds_read_b128 v[156:159], v228 offset:31488\n"
      "ds_read_b128 v[160:163], v228 offset:31744\n"
      "ds_read_b128 v[164:167], v228 offset:32000\n"
      ".Lfr2:\n"
      "buffer_load_dwordx4 v227, s[40:43], m0 offen offset:2048 nt lds\n"
      "s_addk_i32 m0, 24576\n"
      "s_waitcnt vmcnt(6)\n"
      "buffer_load_dwordx4 v227, s[40:43], m0 offen offset:0 nt lds\n"
      "s_waitcnt vmcnt(6)\n"
      "buffer_load_dwordx4 v227, s[40:43], m0 offen offset:1024 nt lds\n"
      "s_waitcnt vmcnt(6)\n"
      "buffer_load_dwordx4 v227, s[40:43], m0 offen offset:2048 nt lds\n"
      "s_waitcnt vmcnt(6) lgkmcnt(0)\n"
      "s_barrier\n"
      "s_lshr_b32 s71, %[w], 1\n"
      "s_cmp_lg_u32 s71, 0\n"
      "s_cbranch_scc1 .Lfr3\n"
      "ds_read_b128 v[168:171], v228 offset:32256\n"
      "ds_read_b128 v[172:175], v228 offset:32512\n"
      "ds_read_b128 v[176:179], v228 offset:32768\n"
      "ds_read_b128 v[180:183], v228 offset:33024\n"
      "ds_read_b128 v[184:187], v228 offset:33280\n"
      "ds_read_b128 v[188:191], v228 offset:33536\n"
      "ds_read_b128 v[192:195], v228 offset:33792\n"
      "ds_read_b128 v[196:199], v228 offset:34048\n"
      "ds_read_b128 v[200:203], v228 offset:34304\n"
      "ds_read_b128 v[204:207], v228 offset:34560\n"
      "ds_read_b128 v[208:211], v228 offset:34816\n"
      "ds_read_b128 v[212:215], v228 offset:35072\n"
      "ds_read_b128 v[216:219], v228 offset:35328\n"
      "ds_read_b128 v[220:223], v228 offset:35584\n"
      ".Lfr3:\n"
      "buffer_load_dwordx4 v227, s[40:43], m0 offen offset:3072 nt lds\n"
      "s_addk_i32 m0, 4096\n"
      "s_waitcnt vmcnt(6)\n"
      "buffer_load_dwordx4 v227, s[40:43], m0 offen offset:0 nt lds\n"
      "s_waitcnt vmcnt(6)\n"
      "buffer_load_dwordx4 v227, s[40:43], m0 offen offset:1024 nt lds\n"
      "s_waitcnt vmcnt(6) lgkmcnt(0)\n"
      "s_barrier\n"
      "s_lshr_b32 s71, %[w], 1\n"
      "s_cmp_lg_u32 s71, 1\n"
      "s_cbranch_scc1 .Lfr4\n"
      "ds_read_b128 v[0:3], v228 offset:0\n"
      "ds_read_b128 v[4:7], v228 offset:256\n"
      "ds_read_b128 v[8:11], v228 offset:512\n"
      "ds_read_b128 v[12:15], v228 offset:768\n"
      "ds_read_b128 v[16:19], v228 offset:1024\n"
      "ds_read_b128 v[20:23], v228 offset:1280\n"
      "ds_read_b128 v[24:27], v228 offset:1536\n"
      "ds_read_b128 v[28:31], v228 offset:1792\n"
      "ds_read_b128 v[32:35], v228 offset:2048\n"
      "ds_read_b128 v[36:39], v228 offset:2304\n"
      "ds_read_b128 v[40:43], v228 offset:2560\n"
      "ds_read_b128 v[44:47], v228 offset:2816\n"
      "ds_read_b128 v[48:51], v228 offset:3072\n"
      "ds_read_b128 v[52:55], v228 offset:3328\n"
      ".Lfr4:\n"
      "buffer_load_dwordx4 v227, s[40:43], m0 offen offset:2048 nt lds\n"
      "s_mov_b64 exec, 15\n"
      "buffer_load_dword v231, v230, s[44:47], s39 offen offset:128\n"
      "s_mov_b64 exec, -1\n"
      "s_add_u32 m0, s66, 120832\n"
      "s_movk_i32 s67, 0\n"
      "s_waitcnt vmcnt(7)\n"
      "buffer_load_dwordx4 v229, s[44:47], s67 offen lds\n"
      "s_addk_i32 m0, 4096\n"
      "s_movk_i32 s67, 1792\n"
      "s_waitcnt vmcnt(7)\n"
      "buffer_load_dwordx4 v229, s[44:47], s67 offen lds\n"
      "s_addk_i32 m0, 4096\n"
      "s_mov_b32 s67, 57344\n"
      "s_waitcnt vmcnt(7)\n"
      "buffer_load_dwordx4 v229, s[44:47], s67 offen lds\n"
      "s_waitcnt vmcnt(7) lgkmcnt(0)\n"
      "s_barrier\n"
      "s_lshr_b32 s71, %[w], 1\n"
      "s_cmp_lg_u32 s71, 1\n"
      "s_cbranch_scc1 .Lfr5\n"
      "ds_read_b128 v[56:59], v228 offset:3584\n"
      "ds_read_b128 v[60:63], v228 offset:3840\n"
      "ds_read_b128 v[64:67], v228 offset:4096\n"
      "ds_read_b128 v[68:71], v228 offset:4352\n"
      "ds_read_b128 v[72:75], v228 offset:4608\n"
      "ds_read_b128 v[76:79], v228 offset:4864\n"
      "ds_read_b128 v[80:83], v228 offset:5120\n"
      "ds_read_b128 v[84:87], v228 offset:5376\n"
      "ds_read_b128 v[88:91], v228 offset:5632\n"
      "ds_read_b128 v[92:95], v228 offset:5888\n"
      "ds_read_b128 v[96:99], v228 offset:6144\n"
      "ds_read_b128 v[100:103], v228 offset:6400\n"
      "ds_read_b128 v[104:107], v228 offset:6656\n"
      "ds_read_b128 v[108:111], v228 offset:6912\n"
      ".Lfr5:\n"
      "s_addk_i32 m0, 4096\n"
      "s_addk_i32 s67, 1792\n"
      "buffer_load_dwordx4 v229, s[44:47], s67 offen lds\n"
      "s_addk_i32 m0, 4096\n"
      "s_mov_b32 s67, 114688\n"
      "s_waitcnt vmcnt(7)\n"
      "buffer_load_dwordx4 v229, s[44:47], s67 offen lds\n"
      "s_addk_i32 m0, 4096\n"
      "s_addk_i32 s67, 1792\n"
      "s_waitcnt vmcnt(7)\n"
      "buffer_load_dwordx4 v229, s[44:47], s67 offen lds\n"
      "s_addk_i32 m0, 4096\n"
      "s_mov_b32 s67, 172032\n"
      "s_waitcnt vmcnt(7)\n"
      "buffer_load_dwordx4 v229, s[44:47], s67 offen lds\n"
      "s_waitcnt lgkmcnt(0)\n"
      "s_barrier\n"
      "s_lshr_b32 s71, %[w], 1\n"
      "s_cmp_lg_u32 s71, 1\n"
      "s_cbranch_scc1 .Lfr6\n"
      "ds_read_b128 v[112:115], v228 offset:28672\n"
      "ds_read_b128 v[116:119], v228 offset:28928\n"
      "ds_read_b128 v[120:123], v228 offset:29184\n"
      "ds_read_b128 v[124:127], v228 offset:29440\n"
      "ds_read_b128 v[128:131], v228 offset:29696\n"
      "ds_read_b128 v[132:135], v228 offset:29952\n"
      "ds_read_b128 v[136:139], v228 offset:30208\n"
      "ds_read_b128 v[140:143], v228 offset:30464\n"
      "ds_read_b128 v[144:147], v228 offset:30720\n"
      "ds_read_b128 v[148:151], v228 offset:30976\n"
      "ds_read_b128 v[152:155], v228 offset:31232\n"
      "ds_read_b128 v[156:159], v228 offset:31488\n"
      "ds_read_b128 v[160:163], v228 offset:31744\n"
      "ds_read_b128 v[164:167], v228 offset:32000\n"
      ".Lfr6:\n"
      "s_mov_b32 m0, s66\n"
      "s_addk_i32 s67, 1792\n"
      "buffer_load_dwordx4 v229, s[44:47], s67 offen lds\n"
      "s_addk_i32 m0, 4096\n"
      "s_mov_b32 s67, 229376\n"
      "buffer_load_dwordx4 v229, s[44:47], s67 offen lds\n"
      "s_addk_i32 m0, 4096\n"
      "s_addk_i32 s67, 1792\n"
      "buffer_load_dwordx4 v229, s[44:47], s67 offen lds\n"
      "s_addk_i32 m0, 4096\n"
      "s_mov_b32 s67, 286720\n"
      "buffer_load_dwordx4 v229, s[44:47], s67 offen lds\n"
      "s_addk_i32 m0, 4096\n"
      "s_addk_i32 s67, 1792\n"
      "buffer_load_dwordx4 v229, s[44:47], s67 offen lds\n"
      "v_and_b32 v225, 3, v224\n"
      "v_mul_u32_u24 v225, 896, v225\n"
      "v_lshrrev_b32 v226, 2, v224\n"
      "v_lshl_add_u32 v225, v226, 7, v225\n"
      "s_mov_b64 exec, 0xfffff\n"
      "s_add_u32 m0, s66, 152576\n"
      "s_nop 0\n"
      "buffer_load_dword v225, s[44:47], s39 offen offset:256 lds\n"
      "s_mov_b64 exec, -1\n"
      "s_waitcnt vmcnt(6) lgkmcnt(0)\n"
      "s_branch .Lm2\n"
      ".Lrole_n:\n"
      "v_mov_b32 v249, v224\n"
      "s_mov_b64 s[44:45], %[wp]\n"
      "s_and_b32 s45, s45, 0xffff\n"
      "s_mov_b32 s46, 401408\n"
      "s_mov_b32 s47, 0x20000\n"
      "s_mul_i32 s39, %[sl], 12544\n"
      "s_getpc_b64 s[60:61]\n"
      ".Lpc_here2:\n"
      "s_sub_u32 s60, s60, .Lpc_here2-.Lcode_begin\n"
      "s_subb_u32 s61, s61, 0\n"
      "s_and_b32 s61, s61, 0xffff\n"
      "s_mov_b32 s62, .Lcode_end-.Lcode_begin\n"
      "s_mov_b32 s63, 0x20000\n"
      "s_lshl_b32 s71, %[sl], 3\n"
      "s_add_u32 s71, s71, %[wv]\n"
      "s_lshl_b32 s71, s71, 7\n"
      "v_lshlrev_b32 v250, 15, v249\n"
      "s_mov_b64 exec, 3\n"
      "buffer_load_dword v235, v250, s[60:63], s71 offen\n"
      "s_mov_b64 exec, -1\n"
      "s_setprio 0\n"
      "s_mov_b32 s69, 0xffff0000\n"
      "s_mov_b32 s70, 0x3f803f80\n"
      "s_and_b32 s71, %[w], 2\n"
      "s_lshl_b32 s71, s71, 2\n"
      "v_and_b32 v250, 15, v249\n"
      "v_xor_b32 v250, s71, v250\n"
      "v_lshlrev_b32 v254, 3, v250\n"
      "v_lshrrev_b32 v250, 4, v249\n"
      "v_mul_u32_u24 v252, 896, v250\n"
      "v_add3_u32 v252, v252, v254, s64\n"
      "v_add_u32 v254, 149504, v254\n"
      "v_lshrrev_b32 v250, 5, v249\n"
      "v_bfe_u32 v251, v249, 3, 1\n"
      "v_lshl_add_u32 v244, v250, 4, v251\n"
      "v_bfe_u32 v251, v249, 4, 1\n"
      "v_lshl_add_u32 v244, v251, 2, v244\n"
      "v_mul_u32_u24 v244, 896, v244\n"
      "v_bfe_u32 v250, v249, 1, 2\n"
      "v_bfe_u32 v251, v249, 4, 2\n"
      "v_xor_b32 v250, v250, v251\n"
      "v_lshl_add_u32 v244, v250, 5, v244\n"
      "v_and_b32 v250, 1, v249\n"
      "v_lshl_add_u32 v244, v250, 4, v244\n"
      "s_lshr_b32 s71, %[w], 1\n"
      "s_lshl_b32 s71, s71, 3\n"
      "s_and_b32 s67, %[w], 1\n"
      "s_lshl_b32 s67, s67, 5\n"
      "s_add_u32 s71, s71, s67\n"
      "s_mul_i32 s71, s71, 896\n"
      "v_add_u32 v244, s71, v244\n"
      "s_lshl_b32 s66, %[w], 10\n"
      "s_mul_i32 s71, %[w], 3584\n"
      "s_add_u32 s39, s71, s39\n"
      "v_mul_u32_u24 v255, 896, v249\n"
      "s_mul_i32 s71, %[w], 4352\n"
      "v_bfe_u32 v250, v249, 1, 2\n"
      "v_mul_u32_u24 v246, 528, v250\n"
      "v_and_b32 v250, 1, v249\n"
      "v_lshl_add_u32 v246, v250, 7, v246\n"
      "v_lshrrev_b32 v250, 3, v249\n"
      "v_lshl_add_u32 v246, v250, 2, v246\n"
      "s_add_u32 s71, s71, 57344\n"
      "v_add_u32 v246, s71, v246\n"
      "v_add_u32 v247, 2176, v246\n"
      "v_add_u32 v248, 17408, v246\n"
      "v_add_u32 v239, 19584, v246\n"
      "v_lshrrev_b32 v250, 5, v249\n"
      "v_bfe_u32 v251, v249, 3, 2\n"
      "v_lshl_add_u32 v245, v250, 3, v251\n"
      "v_mul_u32_u24 v245, 896, v245\n"
      "v_and_b32 v250, 7, v249\n"
      "v_lshl_add_u32 v245, v250, 4, v245\n"
      "s_mul_i32 s71, %[w], 28672\n"
      "v_add_u32 v245, s71, v245\n"
      "s_mov_b64 s[48:49], %[op]\n"
      "s_and_b32 s49, s49, 0xffff\n"
      "s_mov_b32 s50, 114688\n"
      "s_mov_b32 s51, 0x20000\n"
      "s_waitcnt vmcnt(1)\n"
      "s_barrier\n"
      "ds_read_b64 v[146:147], v254 offset:0\n"
      "ds_read_b64 v[150:151], v254 offset:128\n"
      "ds_read_b64 v[154:155], v254 offset:256\n"
      "ds_read_b64 v[158:159], v254 offset:384\n"
      "ds_read_b64 v[162:163], v254 offset:512\n"
      "ds_read_b64 v[166:167], v254 offset:640\n"
      "ds_read_b64 v[170:171], v254 offset:768\n"
      "ds_read_b64 v[174:175], v254 offset:1024\n"
      "ds_read_b64 v[178:179], v254 offset:1152\n"
      "ds_read_b64 v[182:183], v254 offset:1280\n"
      "ds_read_b64 v[186:187], v254 offset:1408\n"
      "ds_read_b64 v[190:191], v254 offset:1536\n"
      "ds_read_b64 v[194:195], v254 offset:1664\n"
      "ds_read_b64 v[198:199], v254 offset:1792\n"
      "s_waitcnt lgkmcnt(13)\n"
      "v_lshlrev_b32 v144, 16, v146\n"
      "v_and_b32 v145, s69, v146\n"
      "v_lshlrev_b32 v146, 16, v147\n"
      "v_and_b32 v147, s69, v147\n"
      "s_waitcnt lgkmcnt(12)\n"
      "v_lshlrev_b32 v148, 16, v150\n"
      "v_and_b32 v149, s69, v150\n"
      "v_lshlrev_b32 v150, 16, v151\n"
      "v_and_b32 v151, s69, v151\n"
      "s_waitcnt lgkmcnt(11)\n"
      "v_lshlrev_b32 v152, 16, v154\n"
      "v_and_b32 v153, s69, v154\n"
      "v_lshlrev_b32 v154, 16, v155\n"
      "v_and_b32 v155, s69, v155\n"
      "s_waitcnt lgkmcnt(10)\n"
      "v_lshlrev_b32 v156, 16, v158\n"
      "v_and_b32 v157, s69, v158\n"
      "v_lshlrev_b32 v158, 16, v159\n"
      "v_and_b32 v159, s69, v159\n"
      "s_waitcnt lgkmcnt(9)\n"
      "v_lshlrev_b32 v160, 16, v162\n"
      "v_and_b32 v161, s69, v162\n"
      "v_lshlrev_b32 v162, 16, v163\n"
      "v_and_b32 v163, s69, v163\n"
      "s_waitcnt lgkmcnt(8)\n"
      "v_lshlrev_b32 v164, 16, v166\n"
      "v_and_b32 v165, s69, v166\n"
      "v_lshlrev_b32 v166, 16, v167\n"
      "v_and_b32 v167, s69, v167\n"
      "s_waitcnt lgkmcnt(7)\n"
      "v_lshlrev_b32 v168, 16, v170\n"
      "v_and_b32 v169, s69, v170\n"
      "v_lshlrev_b32 v170, 16, v171\n"
      "v_and_b32 v171, s69, v171\n"
      "s_waitcnt lgkmcnt(6)\n"
      "v_lshlrev_b32 v172, 16, v174\n"
      "v_and_b32 v173, s69, v174\n"
      "v_lshlrev_b32 v174, 16, v175\n"
      "v_and_b32 v175, s69, v175\n"
      "s_waitcnt lgkmcnt(5)\n"
      "v_lshlrev_b32 v176, 16, v178\n"
      "v_and_b32 v177, s69, v178\n"
      "v_lshlrev_b32 v178, 16, v179\n"
      "v_and_b32 v179, s69, v179\n"
      "s_waitcnt lgkmcnt(4)\n"
      "v_lshlrev_b32 v180, 16, v182\n"
      "v_and_b32 v181, s69, v182\n"
      "v_lshlrev_b32 v182, 16, v183\n"
      "v_and_b32 v183, s69, v183\n"
      "s_waitcnt lgkmcnt(3)\n"
      "v_lshlrev_b32 v184, 16, v186\n"
      "v_and_b32 v185, s69, v186\n"
      "v_lshlrev_b32 v186, 16, v187\n"
      "v_and_b32 v187, s69, v187\n"
      "s_waitcnt lgkmcnt(2)\n"
      "v_lshlrev_b32 v188, 16, v190\n"
      "v_and_b32 v189, s69, v190\n"
      "v_lshlrev_b32 v190, 16, v191\n"
      "v_and_b32 v191, s69, v191\n"
      "s_waitcnt lgkmcnt(1)\n"
      "v_lshlrev_b32 v192, 16, v194\n"
      "v_and_b32 v193, s69, v194\n"
      "v_lshlrev_b32 v194, 16, v195\n"
      "v_and_b32 v195, s69, v195\n"
      "s_waitcnt lgkmcnt(0)\n"
      "v_lshlrev_b32 v196, 16, v198\n"
      "v_and_b32 v197, s69, v198\n"
      "v_lshlrev_b32 v198, 16, v199\n"
      "v_and_b32 v199, s69, v199\n"
      "s_barrier\n"
      "ds_read_b64 v[114:115], v252 offset:0\n"
      "ds_read_b64 v[118:119], v252 offset:128\n"
      "ds_read_b64 v[122:123], v252 offset:256\n"
      "ds_read_b64 v[126:127], v252 offset:384\n"
      "ds_read_b64 v[130:131], v252 offset:512\n"
      "ds_read_b64 v[134:135], v252 offset:640\n"
      "ds_read_b64 v[138:139], v252 offset:768\n"
      "s_waitcnt lgkmcnt(6)\n"
      "v_dot2_f32_bf16 v140, v114, s70, 0\n"
      "v_dot2_f32_bf16 v141, v114, v114, 0\n"
      "v_dot2_f32_bf16 v140, v115, s70, v140\n"
      "v_dot2_f32_bf16 v141, v115, v115, v141\n"
      "s_waitcnt lgkmcnt(5)\n"
      "v_dot2_f32_bf16 v140, v118, s70, v140\n"
      "v_dot2_f32_bf16 v141, v118, v118, v141\n"
      "v_dot2_f32_bf16 v140, v119, s70, v140\n"
      "v_dot2_f32_bf16 v141, v119, v119, v141\n"
      "s_waitcnt lgkmcnt(4)\n"
      "v_dot2_f32_bf16 v140, v122, s70, v140\n"
      "v_dot2_f32_bf16 v141, v122, v122, v141\n"
      "v_dot2_f32_bf16 v140, v123, s70, v140\n"
      "v_dot2_f32_bf16 v141, v123, v123, v141\n"
      "s_waitcnt lgkmcnt(3)\n"
      "v_dot2_f32_bf16 v140, v126, s70, v140\n"
      "v_dot2_f32_bf16 v141, v126, v126, v141\n"
      "v_dot2_f32_bf16 v140, v127, s70, v140\n"
      "v_dot2_f32_bf16 v141, v127, v127, v141\n"
      "s_waitcnt lgkmcnt(2)\n"
      "v_dot2_f32_bf16 v140, v130, s70, v140\n"
      "v_dot2_f32_bf16 v141, v130, v130, v141\n"
      "v_dot2_f32_bf16 v140, v131, s70, v140\n"
      "v_dot2_f32_bf16 v141, v131, v131, v141\n"
      "s_waitcnt lgkmcnt(1)\n"
      "v_dot2_f32_bf16 v140, v134, s70, v140\n"
      "v_dot2_f32_bf16 v141, v134, v134, v141\n"
      "v_dot2_f32_bf16 v140, v135, s70, v140\n"
      "v_dot2_f32_bf16 v141, v135, v135, v141\n"
      "s_waitcnt lgkmcnt(0)\n"
      "v_dot2_f32_bf16 v140, v138, s70, v140\n"
      "v_dot2_f32_bf16 v141, v138, v138, v141\n"
      "v_dot2_f32_bf16 v140, v139, s70, v140\n"
      "v_dot2_f32_bf16 v141, v139, v139, v141\n"
      "v_lshlrev_b32 v112, 16, v114\n"
      "v_and_b32 v113, s69, v114\n"
      "v_lshlrev_b32 v114, 16, v115\n"
      "v_and_b32 v115, s69, v115\n"
      "v_add_f32_dpp v140, v140, v140 row_ror:8 row_mask:0xf bank_mask:0xf\n"
      "v_add_f32_dpp v141, v141, v141 row_ror:8 row_mask:0xf bank_mask:0xf\n"
      "v_lshlrev_b32 v116, 16, v118\n"
      "v_and_b32 v117, s69, v118\n"
      "v_add_f32_dpp v140, v140, v140 row_ror:4 row_mask:0xf bank_mask:0xf\n"
      "v_add_f32_dpp v141, v141, v141 row_ror:4 row_mask:0xf bank_mask:0xf\n"
      "v_lshlrev_b32 v118, 16, v119\n"
      "v_and_b32 v119, s69, v119\n"
      "v_add_f32_dpp v140, v140, v140 row_ror:2 row_mask:0xf bank_mask:0xf\n"
      "v_add_f32_dpp v141, v141, v141 row_ror:2 row_mask:0xf bank_mask:0xf\n"
      "v_lshlrev_b32 v120, 16, v122\n"
      "v_and_b32 v121, s69, v122\n"
      "v_add_f32_dpp v140, v140, v140 row_ror:1 row_mask:0xf bank_mask:0xf\n"
      "v_add_f32_dpp v141, v141, v141 row_ror:1 row_mask:0xf bank_mask:0xf\n"
      "v_lshlrev_b32 v122, 16, v123\n"
      "v_and_b32 v123, s69, v123\n"
      "v_mul_f32 v140, 0x3b124925, v140\n"
      "v_lshlrev_b32 v124, 16, v126\n"
      "v_mul_f32 v141, 0x3b124925, v141\n"
      "v_and_b32 v125, s69, v126\n"
      "v_fma_f32 v141, -v140, v140, v141\n"
      "v_lshlrev_b32 v126, 16, v127\n"
      "v_max_f32 v141, 0, v141\n"
      "v_and_b32 v127, s69, v127\n"
      "v_add_f32 v141, 0x3727c5ac, v141\n"
      "v_lshlrev_b32 v128, 16, v130\n"
      "v_rsq_f32 v142, v141\n"
      "v_and_b32 v129, s69, v130\n"
      "v_lshlrev_b32 v130, 16, v131\n"
      "v_and_b32 v131, s69, v131\n"
      "v_lshlrev_b32 v132, 16, v134\n"
      "v_and_b32 v133, s69, v134\n"
      "v_lshlrev_b32 v134, 16, v135\n"
      "v_and_b32 v135, s69, v135\n"
      "v_lshlrev_b32 v136, 16, v138\n"
      "v_and_b32 v137, s69, v138\n"
      "v_lshlrev_b32 v138, 16, v139\n"
      "v_and_b32 v139, s69, v139\n"
      "v_mul_f32_e64 v143, -v140, v142\n"
      ".p2align 3\n"
      "v_pk_fma_f32 v[112:113], v[112:113], v[142:143], v[142:143] op_sel:[0,0,1] op_sel_hi:[1,0,1]\n"
      "v_pk_fma_f32 v[114:115], v[114:115], v[142:143], v[142:143] op_sel:[0,0,1] op_sel_hi:[1,0,1]\n"
      "v_pk_fma_f32 v[112:113], v[112:113], v[144:145], v[172:173]\n"
      "v_pk_fma_f32 v[114:115], v[114:115], v[146:147], v[174:175]\n"
      "v_cvt_pk_bf16_f32 v112, v112, v113\n"
      "v_cvt_pk_bf16_f32 v113, v114, v115\n"
      "v_pk_fma_f32 v[116:117], v[116:117], v[142:143], v[142:143] op_sel:[0,0,1] op_sel_hi:[1,0,1]\n"
      "v_pk_fma_f32 v[118:119], v[118:119], v[142:143], v[142:143] op_sel:[0,0,1] op_sel_hi:[1,0,1]\n"
      "v_pk_fma_f32 v[116:117], v[116:117], v[148:149], v[176:177]\n"
      "v_pk_fma_f32 v[118:119], v[118:119], v[150:151], v[178:179]\n"
      "v_cvt_pk_bf16_f32 v116, v116, v117\n"
      "v_cvt_pk_bf16_f32 v117, v118, v119\n"
      "s_mov_b32 m0, s64\n"
      "s_nop 0\n"
      "ds_write_addtid_b32 v112 offset:0\n"
      "ds_write_addtid_b32 v113 offset:256\n"
      "v_pk_fma_f32 v[120:121], v[120:121], v[142:143], v[142:143] op_sel:[0,0,1] op_sel_hi:[1,0,1]\n"
      "v_pk_fma_f32 v[122:123], v[122:123], v[142:143], v[142:143] op_sel:[0,0,1] op_sel_hi:[1,0,1]\n"
      "v_pk_fma_f32 v[120:121], v[120:121], v[152:153], v[180:181]\n"
      "v_pk_fma_f32 v[122:123], v[122:123], v[154:155], v[182:183]\n"
      "v_cvt_pk_bf16_f32 v120, v120, v121\n"
      "v_cvt_pk_bf16_f32 v121, v122, v123\n"
      "ds_write_addtid_b32 v116 offset:512\n"
      "ds_write_addtid_b32 v117 offset:768\n"
      "v_pk_fma_f32 v[124:125], v[124:125], v[142:143], v[142:143] op_sel:[0,0,1] op_sel_hi:[1,0,1]\n"
      "v_pk_fma_f32 v[126:127], v[126:127], v[142:143], v[142:143] op_sel:[0,0,1] op_sel_hi:[1,0,1]\n"
      "v_pk_fma_f32 v[124:125], v[124:125], v[156:157], v[184:185]\n"
      "v_pk_fma_f32 v[126:127], v[126:127], v[158:159], v[186:187]\n"
      "v_cvt_pk_bf16_f32 v124, v124, v125\n"
      "v_cvt_pk_bf16_f32 v125, v126, v127\n"
      "ds_write_addtid_b32 v120 offset:1024\n"
      "ds_write_addtid_b32 v121 offset:1280\n"
      "v_pk_fma_f32 v[128:129], v[128:129], v[142:143], v[142:143] op_sel:[0,0,1] op_sel_hi:[1,0,1]\n"
      "v_pk_fma_f32 v[130:131], v[130:131], v[142:143], v[142:143] op_sel:[0,0,1] op_sel_hi:[1,0,1]\n"
      "v_pk_fma_f32 v[128:129], v[128:129], v[160:161], v[188:189]\n"
      "v_pk_fma_f32 v[130:131], v[130:131], v[162:163], v[190:191]\n"
      "v_cvt_pk_bf16_f32 v128, v128, v129\n"
      "v_cvt_pk_bf16_f32 v129, v130, v131\n"
      "ds_write_addtid_b32 v124 offset:1536\n"
      "ds_write_addtid_b32 v125 offset:1792\n"
      "v_pk_fma_f32 v[132:133], v[132:133], v[142:143], v[142:143] op_sel:[0,0,1] op_sel_hi:[1,0,1]\n"
      "v_pk_fma_f32 v[134:135], v[134:135], v[142:143], v[142:143] op_sel:[0,0,1] op_sel_hi:[1,0,1]\n"
      "v_pk_fma_f32 v[132:133], v[132:133], v[164:165], v[192:193]\n"
      "v_pk_fma_f32 v[134:135], v[134:135], v[166:167], v[194:195]\n"
      "v_cvt_pk_bf16_f32 v132, v132, v133\n"
      "v_cvt_pk_bf16_f32 v133, v134, v135\n"
      "ds_write_addtid_b32 v128 offset:2048\n"
      "ds_write_addtid_b32 v129 offset:2304\n"
      "v_pk_fma_f32 v[136:137], v[136:137], v[142:143], v[142:143] op_sel:[0,0,1] op_sel_hi:[1,0,1]\n"
      "v_pk_fma_f32 v[138:139], v[138:139], v[142:143], v[142:143] op_sel:[0,0,1] op_sel_hi:[1,0,1]\n"
      "v_pk_fma_f32 v[136:137], v[136:137], v[168:169], v[196:197]\n"
      "v_pk_fma_f32 v[138:139], v[138:139], v[170:171], v[198:199]\n"
      "v_cvt_pk_bf16_f32 v136, v136, v137\n"
      "v_cvt_pk_bf16_f32 v137, v138, v139\n"
      "ds_write_addtid_b32 v132 offset:2560\n"
      "ds_write_addtid_b32 v133 offset:2816\n"
      "ds_write_addtid_b32 v136 offset:3072\n"
      "ds_write_addtid_b32 v137 offset:3328\n"
      "s_waitcnt lgkmcnt(0)\n"
      "s_barrier\n"
      "ds_read_b64 v[114:115], v252 offset:3584\n"
      "ds_read_b64 v[118:119], v252 offset:3712\n"
      "ds_read_b64 v[122:123], v252 offset:3840\n"
      "ds_read_b64 v[126:127], v252 offset:3968\n"
      "ds_read_b64 v[130:131], v252 offset:4096\n"
      "ds_read_b64 v[134:135], v252 offset:4224\n"
      "ds_read_b64 v[138:139], v252 offset:4352\n"
      "s_waitcnt lgkmcnt(6)\n"
      "v_dot2_f32_bf16 v140, v114, s70, 0\n"
      "v_dot2_f32_bf16 v141, v114, v114, 0\n"
      "v_dot2_f32_bf16 v140, v115, s70, v140\n"
      "v_dot2_f32_bf16 v141, v115, v115, v141\n"
      "s_waitcnt lgkmcnt(5)\n"
      "v_dot2_f32_bf16 v140, v118, s70, v140\n"
      "v_dot2_f32_bf16 v141, v118, v118, v141\n"
      "v_dot2_f32_bf16 v140, v119, s70, v140\n"
      "v_dot2_f32_bf16 v141, v119, v119, v141\n"
      "s_waitcnt lgkmcnt(4)\n"
      "v_dot2_f32_bf16 v140, v122, s70, v140\n"
      "v_dot2_f32_bf16 v141, v122, v122, v141\n"
      "v_dot2_f32_bf16 v140, v123, s70, v140\n"
      "v_dot2_f32_bf16 v141, v123, v123, v141\n"
      "s_waitcnt lgkmcnt(3)\n"
      "v_dot2_f32_bf16 v140, v126, s70, v140\n"
      "v_dot2_f32_bf16 v141, v126, v126, v141\n"
      "v_dot2_f32_bf16 v140, v127, s70, v140\n"
      "v_dot2_f32_bf16 v141, v127, v127, v141\n"
      "s_waitcnt lgkmcnt(2)\n"
      "v_dot2_f32_bf16 v140, v130, s70, v140\n"
      "v_dot2_f32_bf16 v141, v130, v130, v141\n"
      "v_dot2_f32_bf16 v140, v131, s70, v140\n"
      "v_dot2_f32_bf16 v141, v131, v131, v141\n"
      "s_waitcnt lgkmcnt(1)\n"
      "v_dot2_f32_bf16 v140, v134, s70, v140\n"
      "v_dot2_f32_bf16 v141, v134, v134, v141\n"
      "v_dot2_f32_bf16 v140, v135, s70, v140\n"
      "v_dot2_f32_bf16 v141, v135, v135, v141\n"
      "s_waitcnt lgkmcnt(0)\n"
      "v_dot2_f32_bf16 v140, v138, s70, v140\n"
      "v_dot2_f32_bf16 v141, v138, v138, v141\n"
      "v_dot2_f32_bf16 v140, v139, s70, v140\n"
      "v_dot2_f32_bf16 v141, v139, v139, v141\n"
      "v_lshlrev_b32 v112, 16, v114\n"
      "v_and_b32 v113, s69, v114\n"
      "v_lshlrev_b32 v114, 16, v115\n"
      "v_and_b32 v115, s69, v115\n"
      "v_add_f32_dpp v140, v140, v140 row_ror:8 row_mask:0xf bank_mask:0xf\n"
      "v_add_f32_dpp v141, v141, v141 row_ror:8 row_mask:0xf bank_mask:0xf\n"
      "v_lshlrev_b32 v116, 16, v118\n"
      "v_and_b32 v117, s69, v118\n"
      "v_add_f32_dpp v140, v140, v140 row_ror:4 row_mask:0xf bank_mask:0xf\n"
      "v_add_f32_dpp v141, v141, v141 row_ror:4 row_mask:0xf bank_mask:0xf\n"
      "v_lshlrev_b32 v118, 16, v119\n"
      "v_and_b32 v119, s69, v119\n"
      "v_add_f32_dpp v140, v140, v140 row_ror:2 row_mask:0xf bank_mask:0xf\n"
      "v_add_f32_dpp v141, v141, v141 row_ror:2 row_mask:0xf bank_mask:0xf\n"
      "v_lshlrev_b32 v120, 16, v122\n"
      "v_and_b32 v121, s69, v122\n"
      "v_add_f32_dpp v140, v140, v140 row_ror:1 row_mask:0xf bank_mask:0xf\n"
      "v_add_f32_dpp v141, v141, v141 row_ror:1 row_mask:0xf bank_mask:0xf\n"
      "v_lshlrev_b32 v122, 16, v123\n"
      "v_and_b32 v123, s69, v123\n"
      "v_mul_f32 v140, 0x3b124925, v140\n"
      "v_lshlrev_b32 v124, 16, v126\n"
      "v_mul_f32 v141, 0x3b124925, v141\n"
      "v_and_b32 v125, s69, v126\n"
      "v_fma_f32 v141, -v140, v140, v141\n"
      "v_lshlrev_b32 v126, 16, v127\n"
      "v_max_f32 v141, 0, v141\n"
      "v_and_b32 v127, s69, v127\n"
      "v_add_f32 v141, 0x3727c5ac, v141\n"
      "v_lshlrev_b32 v128, 16, v130\n"
      "v_rsq_f32 v142, v141\n"
      "v_and_b32 v129, s69, v130\n"
      "v_lshlrev_b32 v130, 16, v131\n"
      "v_and_b32 v131, s69, v131\n"
      "v_lshlrev_b32 v132, 16, v134\n"
      "v_and_b32 v133, s69, v134\n"
      "v_lshlrev_b32 v134, 16, v135\n"
      "v_and_b32 v135, s69, v135\n"
      "v_lshlrev_b32 v136, 16, v138\n"
      "v_and_b32 v137, s69, v138\n"
      "v_lshlrev_b32 v138, 16, v139\n"
      "v_and_b32 v139, s69, v139\n"
      "v_mul_f32_e64 v143, -v140, v142\n"
      ".p2align 3\n"
      "v_pk_fma_f32 v[112:113], v[112:113], v[142:143], v[142:143] op_sel:[0,0,1] op_sel_hi:[1,0,1]\n"
      "v_pk_fma_f32 v[114:115], v[114:115], v[142:143], v[142:143] op_sel:[0,0,1] op_sel_hi:[1,0,1]\n"
      "v_pk_fma_f32 v[112:113], v[112:113], v[144:145], v[172:173]\n"
      "v_pk_fma_f32 v[114:115], v[114:115], v[146:147], v[174:175]\n"
      "v_cvt_pk_bf16_f32 v112, v112, v113\n"
      "v_cvt_pk_bf16_f32 v113, v114, v115\n"
      "v_pk_fma_f32 v[116:117], v[116:117], v[142:143], v[142:143] op_sel:[0,0,1] op_sel_hi:[1,0,1]\n"
      "v_pk_fma_f32 v[118:119], v[118:119], v[142:143], v[142:143] op_sel:[0,0,1] op_sel_hi:[1,0,1]\n"
      "v_pk_fma_f32 v[116:117], v[116:117], v[148:149], v[176:177]\n"
      "v_pk_fma_f32 v[118:119], v[118:119], v[150:151], v[178:179]\n"
      "v_cvt_pk_bf16_f32 v116, v116, v117\n"
      "v_cvt_pk_bf16_f32 v117, v118, v119\n"
      "ds_write_addtid_b32 v112 offset:3584\n"
      "ds_write_addtid_b32 v113 offset:3840\n"
      "v_pk_fma_f32 v[120:121], v[120:121], v[142:143], v[142:143] op_sel:[0,0,1] op_sel_hi:[1,0,1]\n"
      "v_pk_fma_f32 v[122:123], v[122:123], v[142:143], v[142:143] op_sel:[0,0,1] op_sel_hi:[1,0,1]\n"
      "v_pk_fma_f32 v[120:121], v[120:121], v[152:153], v[180:181]\n"
      "v_pk_fma_f32 v[122:123], v[122:123], v[154:155], v[182:183]\n"
      "v_cvt_pk_bf16_f32 v120, v120, v121\n"
      "v_cvt_pk_bf16_f32 v121, v122, v123\n"
      "ds_write_addtid_b32 v116 offset:4096\n"
      "ds_write_addtid_b32 v117 offset:4352\n"
      "v_pk_fma_f32 v[124:125], v[124:125], v[142:143], v[142:143] op_sel:[0,0,1] op_sel_hi:[1,0,1]\n"
      "v_pk_fma_f32 v[126:127], v[126:127], v[142:143], v[142:143] op_sel:[0,0,1] op_sel_hi:[1,0,1]\n"
      "v_pk_fma_f32 v[124:125], v[124:125], v[156:157], v[184:185]\n"
      "v_pk_fma_f32 v[126:127], v[126:127], v[158:159], v[186:187]\n"
      "v_cvt_pk_bf16_f32 v124, v124, v125\n"
      "v_cvt_pk_bf16_f32 v125, v126, v127\n"
      "ds_write_addtid_b32 v120 offset:4608\n"
      "ds_write_addtid_b32 v121 offset:4864\n"
      "v_pk_fma_f32 v[128:129], v[128:129], v[142:143], v[142:143] op_sel:[0,0,1] op_sel_hi:[1,0,1]\n"
      "v_pk_fma_f32 v[130:131], v[130:131], v[142:143], v[142:143] op_sel:[0,0,1] op_sel_hi:[1,0,1]\n"
      "v_pk_fma_f32 v[128:129], v[128:129], v[160:161], v[188:189]\n"
      "v_pk_fma_f32 v[130:131], v[130:131], v[162:163], v[190:191]\n"
      "v_cvt_pk_bf16_f32 v128, v128, v129\n"
      "v_cvt_pk_bf16_f32 v129, v130, v131\n"
      "ds_write_addtid_b32 v124 offset:5120\n"
      "ds_write_addtid_b32 v125 offset:5376\n"
      "v_pk_fma_f32 v[132:133], v[132:133], v[142:143], v[142:143] op_sel:[0,0,1] op_sel_hi:[1,0,1]\n"
      "v_pk_fma_f32 v[134:135], v[134:135], v[142:143], v[142:143] op_sel:[0,0,1] op_sel_hi:[1,0,1]\n"
      "v_pk_fma_f32 v[132:133], v[132:133], v[164:165], v[192:193]\n"
      "v_pk_fma_f32 v[134:135], v[134:135], v[166:167], v[194:195]\n"
      "v_cvt_pk_bf16_f32 v132, v132, v133\n"
      "v_cvt_pk_bf16_f32 v133, v134, v135\n"
      "ds_write_addtid_b32 v128 offset:5632\n"
      "ds_write_addtid_b32 v129 offset:5888\n"
      "v_pk_fma_f32 v[136:137], v[136:137], v[142:143], v[142:143] op_sel:[0,0,1] op_sel_hi:[1,0,1]\n"
      "v_pk_fma_f32 v[138:139], v[138:139], v[142:143], v[142:143] op_sel:[0,0,1] op_sel_hi:[1,0,1]\n"
      "v_pk_fma_f32 v[136:137], v[136:137], v[168:169], v[196:197]\n"
      "v_pk_fma_f32 v[138:139], v[138:139], v[170:171], v[198:199]\n"
      "v_cvt_pk_bf16_f32 v136, v136, v137\n"
      "v_cvt_pk_bf16_f32 v137, v138, v139\n"
      "ds_write_addtid_b32 v132 offset:6144\n"
      "ds_write_addtid_b32 v133 offset:6400\n"
      "ds_write_addtid_b32 v136 offset:6656\n"
      "ds_write_addtid_b32 v137 offset:6912\n"
      "s_waitcnt lgkmcnt(0)\n"
      "s_barrier\n"
      "ds_read_b64 v[114:115], v252 offset:28672\n"
      "ds_read_b64 v[118:119], v252 offset:28800\n"
      "ds_read_b64 v[122:123], v252 offset:28928\n"
      "ds_read_b64 v[126:127], v252 offset:29056\n"
      "ds_read_b64 v[130:131], v252 offset:29184\n"
      "ds_read_b64 v[134:135], v252 offset:29312\n"
      "ds_read_b64 v[138:139], v252 offset:29440\n"
      "s_waitcnt lgkmcnt(6)\n"
      "v_dot2_f32_bf16 v140, v114, s70, 0\n"
      "v_dot2_f32_bf16 v141, v114, v114, 0\n"
      "v_dot2_f32_bf16 v140, v115, s70, v140\n"
      "v_dot2_f32_bf16 v141, v115, v115, v141\n"
      "s_waitcnt lgkmcnt(5)\n"
      "v_dot2_f32_bf16 v140, v118, s70, v140\n"
      "v_dot2_f32_bf16 v141, v118, v118, v141\n"
      "v_dot2_f32_bf16 v140, v119, s70, v140\n"
      "v_dot2_f32_bf16 v141, v119, v119, v141\n"
      "s_waitcnt lgkmcnt(4)\n"
      "v_dot2_f32_bf16 v140, v122, s70, v140\n"
      "v_dot2_f32_bf16 v141, v122, v122, v141\n"
      "v_dot2_f32_bf16 v140, v123, s70, v140\n"
      "v_dot2_f32_bf16 v141, v123, v123, v141\n"
      "s_waitcnt lgkmcnt(3)\n"
      "v_dot2_f32_bf16 v140, v126, s70, v140\n"
      "v_dot2_f32_bf16 v141, v126, v126, v141\n"
      "v_dot2_f32_bf16 v140, v127, s70, v140\n"
      "v_dot2_f32_bf16 v141, v127, v127, v141\n"
      "s_waitcnt lgkmcnt(2)\n"
      "v_dot2_f32_bf16 v140, v130, s70, v140\n"
      "v_dot2_f32_bf16 v141, v130, v130, v141\n"
      "v_dot2_f32_bf16 v140, v131, s70, v140\n"
      "v_dot2_f32_bf16 v141, v131, v131, v141\n"
      "s_waitcnt lgkmcnt(1)\n"
      "v_dot2_f32_bf16 v140, v134, s70, v140\n"
      "v_dot2_f32_bf16 v141, v134, v134, v141\n"
      "v_dot2_f32_bf16 v140, v135, s70, v140\n"
      "v_dot2_f32_bf16 v141, v135, v135, v141\n"
      "s_waitcnt lgkmcnt(0)\n"
      "v_dot2_f32_bf16 v140, v138, s70, v140\n"
      "v_dot2_f32_bf16 v141, v138, v138, v141\n"
      "v_dot2_f32_bf16 v140, v139, s70, v140\n"
      "v_dot2_f32_bf16 v141, v139, v139, v141\n"
      "v_lshlrev_b32 v112, 16, v114\n"
      "v_and_b32 v113, s69, v114\n"
      "v_lshlrev_b32 v114, 16, v115\n"
      "v_and_b32 v115, s69, v115\n"
      "v_add_f32_dpp v140, v140, v140 row_ror:8 row_mask:0xf bank_mask:0xf\n"
      "v_add_f32_dpp v141, v141, v141 row_ror:8 row_mask:0xf bank_mask:0xf\n"
      "v_lshlrev_b32 v116, 16, v118\n"
      "v_and_b32 v117, s69, v118\n"
      "v_add_f32_dpp v140, v140, v140 row_ror:4 row_mask:0xf bank_mask:0xf\n"
      "v_add_f32_dpp v141, v141, v141 row_ror:4 row_mask:0xf bank_mask:0xf\n"
      "v_lshlrev_b32 v118, 16, v119\n"
      "v_and_b32 v119, s69, v119\n"
      "v_add_f32_dpp v140, v140, v140 row_ror:2 row_mask:0xf bank_mask:0xf\n"
      "v_add_f32_dpp v141, v141, v141 row_ror:2 row_mask:0xf bank_mask:0xf\n"
      "v_lshlrev_b32 v120, 16, v122\n"
      "v_and_b32 v121, s69, v122\n"
      "v_add_f32_dpp v140, v140, v140 row_ror:1 row_mask:0xf bank_mask:0xf\n"
      "v_add_f32_dpp v141, v141, v141 row_ror:1 row_mask:0xf bank_mask:0xf\n"
      "v_lshlrev_b32 v122, 16, v123\n"
      "v_and_b32 v123, s69, v123\n"
      "v_mul_f32 v140, 0x3b124925, v140\n"
      "v_lshlrev_b32 v124, 16, v126\n"
      "v_mul_f32 v141, 0x3b124925, v141\n"
      "v_and_b32 v125, s69, v126\n"
      "v_fma_f32 v141, -v140, v140, v141\n"
      "v_lshlrev_b32 v126, 16, v127\n"
      "v_max_f32 v141, 0, v141\n"
      "v_and_b32 v127, s69, v127\n"
      "v_add_f32 v141, 0x3727c5ac, v141\n"
      "v_lshlrev_b32 v128, 16, v130\n"
      "v_rsq_f32 v142, v141\n"
      "v_and_b32 v129, s69, v130\n"
      "v_lshlrev_b32 v130, 16, v131\n"
      "v_and_b32 v131, s69, v131\n"
      "v_lshlrev_b32 v132, 16, v134\n"
      "v_and_b32 v133, s69, v134\n"
      "v_lshlrev_b32 v134, 16, v135\n"
      "v_and_b32 v135, s69, v135\n"
      "v_lshlrev_b32 v136, 16, v138\n"
      "v_and_b32 v137, s69, v138\n"
      "v_lshlrev_b32 v138, 16, v139\n"
      "v_and_b32 v139, s69, v139\n"
      "v_mul_f32_e64 v143, -v140, v142\n"
      ".p2align 3\n"
      "v_pk_fma_f32 v[112:113], v[112:113], v[142:143], v[142:143] op_sel:[0,0,1] op_sel_hi:[1,0,1]\n"
      "v_pk_fma_f32 v[114:115], v[114:115], v[142:143], v[142:143] op_sel:[0,0,1] op_sel_hi:[1,0,1]\n"
      "v_pk_fma_f32 v[112:113], v[112:113], v[144:145], v[172:173]\n"
      "v_pk_fma_f32 v[114:115], v[114:115], v[146:147], v[174:175]\n"
      "v_cvt_pk_bf16_f32 v112, v112, v113\n"
      "v_cvt_pk_bf16_f32 v113, v114, v115\n"
      "v_pk_fma_f32 v[116:117], v[116:117], v[142:143], v[142:143] op_sel:[0,0,1] op_sel_hi:[1,0,1]\n"
      "v_pk_fma_f32 v[118:119], v[118:119], v[142:143], v[142:143] op_sel:[0,0,1] op_sel_hi:[1,0,1]\n"
      "v_pk_fma_f32 v[116:117], v[116:117], v[148:149], v[176:177]\n"
      "v_pk_fma_f32 v[118:119], v[118:119], v[150:151], v[178:179]\n"
      "v_cvt_pk_bf16_f32 v116, v116, v117\n"
      "v_cvt_pk_bf16_f32 v117, v118, v119\n"
      "ds_write_addtid_b32 v112 offset:28672\n"
      "ds_write_addtid_b32 v113 offset:28928\n"
      "v_pk_fma_f32 v[120:121], v[120:121], v[142:143], v[142:143] op_sel:[0,0,1] op_sel_hi:[1,0,1]\n"
      "v_pk_fma_f32 v[122:123], v[122:123], v[142:143], v[142:143] op_sel:[0,0,1] op_sel_hi:[1,0,1]\n"
      "v_pk_fma_f32 v[120:121], v[120:121], v[152:153], v[180:181]\n"
      "v_pk_fma_f32 v[122:123], v[122:123], v[154:155], v[182:183]\n"
      "v_cvt_pk_bf16_f32 v120, v120, v121\n"
      "v_cvt_pk_bf16_f32 v121, v122, v123\n"
      "ds_write_addtid_b32 v116 offset:29184\n"
      "ds_write_addtid_b32 v117 offset:29440\n"
      "v_pk_fma_f32 v[124:125], v[124:125], v[142:143], v[142:143] op_sel:[0,0,1] op_sel_hi:[1,0,1]\n"
      "v_pk_fma_f32 v[126:127], v[126:127], v[142:143], v[142:143] op_sel:[0,0,1] op_sel_hi:[1,0,1]\n"
      "v_pk_fma_f32 v[124:125], v[124:125], v[156:157], v[184:185]\n"
      "v_pk_fma_f32 v[126:127], v[126:127], v[158:159], v[186:187]\n"
      "v_cvt_pk_bf16_f32 v124, v124, v125\n"
      "v_cvt_pk_bf16_f32 v125, v126, v127\n"
      "ds_write_addtid_b32 v120 offset:29696\n"
      "ds_write_addtid_b32 v121 offset:29952\n"
      "v_pk_fma_f32 v[128:129], v[128:129], v[142:143], v[142:143] op_sel:[0,0,1] op_sel_hi:[1,0,1]\n"
      "v_pk_fma_f32 v[130:131], v[130:131], v[142:143], v[142:143] op_sel:[0,0,1] op_sel_hi:[1,0,1]\n"
      "v_pk_fma_f32 v[128:129], v[128:129], v[160:161], v[188:189]\n"
      "v_pk_fma_f32 v[130:131], v[130:131], v[162:163], v[190:191]\n"
      "v_cvt_pk_bf16_f32 v128, v128, v129\n"
      "v_cvt_pk_bf16_f32 v129, v130, v131\n"
      "ds_write_addtid_b32 v124 offset:30208\n"
      "ds_write_addtid_b32 v125 offset:30464\n"
      "v_pk_fma_f32 v[132:133], v[132:133], v[142:143], v[142:143] op_sel:[0,0,1] op_sel_hi:[1,0,1]\n"
      "v_pk_fma_f32 v[134:135], v[134:135], v[142:143], v[142:143] op_sel:[0,0,1] op_sel_hi:[1,0,1]\n"
      "v_pk_fma_f32 v[132:133], v[132:133], v[164:165], v[192:193]\n"
      "v_pk_fma_f32 v[134:135], v[134:135], v[166:167], v[194:195]\n"
      "v_cvt_pk_bf16_f32 v132, v132, v133\n"
      "v_cvt_pk_bf16_f32 v133, v134, v135\n"
      "ds_write_addtid_b32 v128 offset:30720\n"
      "ds_write_addtid_b32 v129 offset:30976\n"
      "v_pk_fma_f32 v[136:137], v[136:137], v[142:143], v[142:143] op_sel:[0,0,1] op_sel_hi:[1,0,1]\n"
      "v_pk_fma_f32 v[138:139], v[138:139], v[142:143], v[142:143] op_sel:[0,0,1] op_sel_hi:[1,0,1]\n"
      "v_pk_fma_f32 v[136:137], v[136:137], v[168:169], v[196:197]\n"
      "v_pk_fma_f32 v[138:139], v[138:139], v[170:171], v[198:199]\n"
      "v_cvt_pk_bf16_f32 v136, v136, v137\n"
      "v_cvt_pk_bf16_f32 v137, v138, v139\n"
      "ds_write_addtid_b32 v132 offset:31232\n"
      "ds_write_addtid_b32 v133 offset:31488\n"
      "ds_write_addtid_b32 v136 offset:31744\n"
      "ds_write_addtid_b32 v137 offset:32000\n"
      "s_waitcnt lgkmcnt(0)\n"
      "s_barrier\n"
      "ds_read_b64 v[114:115], v252 offset:32256\n"
      "ds_read_b64 v[118:119], v252 offset:32384\n"
      "ds_read_b64 v[122:123], v252 offset:32512\n"
      "ds_read_b64 v[126:127], v252 offset:32640\n"
      "ds_read_b64 v[130:131], v252 offset:32768\n"
      "ds_read_b64 v[134:135], v252 offset:32896\n"
      "ds_read_b64 v[138:139], v252 offset:33024\n"
      "s_waitcnt lgkmcnt(6)\n"
      "v_dot2_f32_bf16 v140, v114, s70, 0\n"
      "v_dot2_f32_bf16 v141, v114, v114, 0\n"
      "v_dot2_f32_bf16 v140, v115, s70, v140\n"
      "v_dot2_f32_bf16 v141, v115, v115, v141\n"
      "s_waitcnt lgkmcnt(5)\n"
      "v_dot2_f32_bf16 v140, v118, s70, v140\n"
      "v_dot2_f32_bf16 v141, v118, v118, v141\n"
      "v_dot2_f32_bf16 v140, v119, s70, v140\n"
      "v_dot2_f32_bf16 v141, v119, v119, v141\n"
      "s_waitcnt lgkmcnt(4)\n"
      "v_dot2_f32_bf16 v140, v122, s70, v140\n"
      "v_dot2_f32_bf16 v141, v122, v122, v141\n"
      "v_dot2_f32_bf16 v140, v123, s70, v140\n"
      "v_dot2_f32_bf16 v141, v123, v123, v141\n"
      "s_waitcnt lgkmcnt(3)\n"
      "v_dot2_f32_bf16 v140, v126, s70, v140\n"
      "v_dot2_f32_bf16 v141, v126, v126, v141\n"
      "v_dot2_f32_bf16 v140, v127, s70, v140\n"
      "v_dot2_f32_bf16 v141, v127, v127, v141\n"
      "s_waitcnt lgkmcnt(2)\n"
      "v_dot2_f32_bf16 v140, v130, s70, v140\n"
      "v_dot2_f32_bf16 v141, v130, v130, v141\n"
      "v_dot2_f32_bf16 v140, v131, s70, v140\n"
      "v_dot2_f32_bf16 v141, v131, v131, v141\n"
      "s_waitcnt lgkmcnt(1)\n"
      "v_dot2_f32_bf16 v140, v134, s70, v140\n"
      "v_dot2_f32_bf16 v141, v134, v134, v141\n"
      "v_dot2_f32_bf16 v140, v135, s70, v140\n"
      "v_dot2_f32_bf16 v141, v135, v135, v141\n"
      "s_waitcnt lgkmcnt(0)\n"
      "v_dot2_f32_bf16 v140, v138, s70, v140\n"
      "v_dot2_f32_bf16 v141, v138, v138, v141\n"
      "v_dot2_f32_bf16 v140, v139, s70, v140\n"
      "v_dot2_f32_bf16 v141, v139, v139, v141\n"
      "v_lshlrev_b32 v112, 16, v114\n"
      "v_and_b32 v113, s69, v114\n"
      "v_lshlrev_b32 v114, 16, v115\n"
      "v_and_b32 v115, s69, v115\n"
      "v_add_f32_dpp v140, v140, v140 row_ror:8 row_mask:0xf bank_mask:0xf\n"
      "v_add_f32_dpp v141, v141, v141 row_ror:8 row_mask:0xf bank_mask:0xf\n"
      "v_lshlrev_b32 v116, 16, v118\n"
      "v_and_b32 v117, s69, v118\n"
      "v_add_f32_dpp v140, v140, v140 row_ror:4 row_mask:0xf bank_mask:0xf\n"
      "v_add_f32_dpp v141, v141, v141 row_ror:4 row_mask:0xf bank_mask:0xf\n"
      "v_lshlrev_b32 v118, 16, v119\n"
      "v_and_b32 v119, s69, v119\n"
      "v_add_f32_dpp v140, v140, v140 row_ror:2 row_mask:0xf bank_mask:0xf\n"
      "v_add_f32_dpp v141, v141, v141 row_ror:2 row_mask:0xf bank_mask:0xf\n"
      "v_lshlrev_b32 v120, 16, v122\n"
      "v_and_b32 v121, s69, v122\n"
      "v_add_f32_dpp v140, v140, v140 row_ror:1 row_mask:0xf bank_mask:0xf\n"
      "v_add_f32_dpp v141, v141, v141 row_ror:1 row_mask:0xf bank_mask:0xf\n"
      "v_lshlrev_b32 v122, 16, v123\n"
      "v_and_b32 v123, s69, v123\n"
      "v_mul_f32 v140, 0x3b124925, v140\n"
      "v_lshlrev_b32 v124, 16, v126\n"
      "v_mul_f32 v141, 0x3b124925, v141\n"
      "v_and_b32 v125, s69, v126\n"
      "v_fma_f32 v141, -v140, v140, v141\n"
      "v_lshlrev_b32 v126, 16, v127\n"
      "v_max_f32 v141, 0, v141\n"
      "v_and_b32 v127, s69, v127\n"
      "v_add_f32 v141, 0x3727c5ac, v141\n"
      "v_lshlrev_b32 v128, 16, v130\n"
      "v_rsq_f32 v142, v141\n"
      "v_and_b32 v129, s69, v130\n"
      "v_lshlrev_b32 v130, 16, v131\n"
      "v_and_b32 v131, s69, v131\n"
      "v_lshlrev_b32 v132, 16, v134\n"
      "v_and_b32 v133, s69, v134\n"
      "v_lshlrev_b32 v134, 16, v135\n"
      "v_and_b32 v135, s69, v135\n"
      "v_lshlrev_b32 v136, 16, v138\n"
      "v_and_b32 v137, s69, v138\n"
      "v_lshlrev_b32 v138, 16, v139\n"
      "v_and_b32 v139, s69, v139\n"
      "v_mul_f32_e64 v143, -v140, v142\n"
      ".p2align 3\n"
      "v_pk_fma_f32 v[112:113], v[112:113], v[142:143], v[142:143] op_sel:[0,0,1] op_sel_hi:[1,0,1]\n"
      "v_pk_fma_f32 v[114:115], v[114:115], v[142:143], v[142:143] op_sel:[0,0,1] op_sel_hi:[1,0,1]\n"
      "v_pk_fma_f32 v[112:113], v[112:113], v[144:145], v[172:173]\n"
      "v_pk_fma_f32 v[114:115], v[114:115], v[146:147], v[174:175]\n"
      "v_cvt_pk_bf16_f32 v112, v112, v113\n"
      "v_cvt_pk_bf16_f32 v113, v114, v115\n"
      "v_pk_fma_f32 v[116:117], v[116:117], v[142:143], v[142:143] op_sel:[0,0,1] op_sel_hi:[1,0,1]\n"
      "v_pk_fma_f32 v[118:119], v[118:119], v[142:143], v[142:143] op_sel:[0,0,1] op_sel_hi:[1,0,1]\n"
      "v_pk_fma_f32 v[116:117], v[116:117], v[148:149], v[176:177]\n"
      "v_pk_fma_f32 v[118:119], v[118:119], v[150:151], v[178:179]\n"
      "v_cvt_pk_bf16_f32 v116, v116, v117\n"
      "v_cvt_pk_bf16_f32 v117, v118, v119\n"
      "ds_write_addtid_b32 v112 offset:32256\n"
      "ds_write_addtid_b32 v113 offset:32512\n"
      "v_pk_fma_f32 v[120:121], v[120:121], v[142:143], v[142:143] op_sel:[0,0,1] op_sel_hi:[1,0,1]\n"
      "v_pk_fma_f32 v[122:123], v[122:123], v[142:143], v[142:143] op_sel:[0,0,1] op_sel_hi:[1,0,1]\n"
      "v_pk_fma_f32 v[120:121], v[120:121], v[152:153], v[180:181]\n"
      "v_pk_fma_f32 v[122:123], v[122:123], v[154:155], v[182:183]\n"
      "v_cvt_pk_bf16_f32 v120, v120, v121\n"
      "v_cvt_pk_bf16_f32 v121, v122, v123\n"
      "ds_write_addtid_b32 v116 offset:32768\n"
      "ds_write_addtid_b32 v117 offset:33024\n"
      "v_pk_fma_f32 v[124:125], v[124:125], v[142:143], v[142:143] op_sel:[0,0,1] op_sel_hi:[1,0,1]\n"
      "v_pk_fma_f32 v[126:127], v[126:127], v[142:143], v[142:143] op_sel:[0,0,1] op_sel_hi:[1,0,1]\n"
      "v_pk_fma_f32 v[124:125], v[124:125], v[156:157], v[184:185]\n"
      "v_pk_fma_f32 v[126:127], v[126:127], v[158:159], v[186:187]\n"
      "v_cvt_pk_bf16_f32 v124, v124, v125\n"
      "v_cvt_pk_bf16_f32 v125, v126, v127\n"
      "ds_write_addtid_b32 v120 offset:33280\n"
      "ds_write_addtid_b32 v121 offset:33536\n"
      "v_pk_fma_f32 v[128:129], v[128:129], v[142:143], v[142:143] op_sel:[0,0,1] op_sel_hi:[1,0,1]\n"
      "v_pk_fma_f32 v[130:131], v[130:131], v[142:143], v[142:143] op_sel:[0,0,1] op_sel_hi:[1,0,1]\n"
      "v_pk_fma_f32 v[128:129], v[128:129], v[160:161], v[188:189]\n"
      "v_pk_fma_f32 v[130:131], v[130:131], v[162:163], v[190:191]\n"
      "v_cvt_pk_bf16_f32 v128, v128, v129\n"
      "v_cvt_pk_bf16_f32 v129, v130, v131\n"
      "ds_write_addtid_b32 v124 offset:33792\n"
      "ds_write_addtid_b32 v125 offset:34048\n"
      "v_pk_fma_f32 v[132:133], v[132:133], v[142:143], v[142:143] op_sel:[0,0,1] op_sel_hi:[1,0,1]\n"
      "v_pk_fma_f32 v[134:135], v[134:135], v[142:143], v[142:143] op_sel:[0,0,1] op_sel_hi:[1,0,1]\n"
      "v_pk_fma_f32 v[132:133], v[132:133], v[164:165], v[192:193]\n"
      "v_pk_fma_f32 v[134:135], v[134:135], v[166:167], v[194:195]\n"
      "v_cvt_pk_bf16_f32 v132, v132, v133\n"
      "v_cvt_pk_bf16_f32 v133, v134, v135\n"
      "ds_write_addtid_b32 v128 offset:34304\n"
      "ds_write_addtid_b32 v129 offset:34560\n"
      "v_pk_fma_f32 v[136:137], v[136:137], v[142:143], v[142:143] op_sel:[0,0,1] op_sel_hi:[1,0,1]\n"
      "v_pk_fma_f32 v[138:139], v[138:139], v[142:143], v[142:143] op_sel:[0,0,1] op_sel_hi:[1,0,1]\n"
      "v_pk_fma_f32 v[136:137], v[136:137], v[168:169], v[196:197]\n"
      "v_pk_fma_f32 v[138:139], v[138:139], v[170:171], v[198:199]\n"
      "v_cvt_pk_bf16_f32 v136, v136, v137\n"
      "v_cvt_pk_bf16_f32 v137, v138, v139\n"
      "ds_write_addtid_b32 v132 offset:34816\n"
      "ds_write_addtid_b32 v133 offset:35072\n"
      "ds_write_addtid_b32 v136 offset:35328\n"
      "ds_write_addtid_b32 v137 offset:35584\n"
      "s_waitcnt lgkmcnt(0)\n"
      "s_barrier\n"
      "v_add_u32 v252, 57344, v252\n"
      "ds_read_b64 v[114:115], v252 offset:0\n"
      "ds_read_b64 v[118:119], v252 offset:128\n"
      "ds_read_b64 v[122:123], v252 offset:256\n"
      "ds_read_b64 v[126:127], v252 offset:384\n"
      "ds_read_b64 v[130:131], v252 offset:512\n"
      "ds_read_b64 v[134:135], v252 offset:640\n"
      "ds_read_b64 v[138:139], v252 offset:768\n"
      "s_waitcnt lgkmcnt(6)\n"
      "v_dot2_f32_bf16 v140, v114, s70, 0\n"
      "v_dot2_f32_bf16 v141, v114, v114, 0\n"
      "v_dot2_f32_bf16 v140, v115, s70, v140\n"
      "v_dot2_f32_bf16 v141, v115, v115, v141\n"
      "s_waitcnt lgkmcnt(5)\n"
      "v_dot2_f32_bf16 v140, v118, s70, v140\n"
      "v_dot2_f32_bf16 v141, v118, v118, v141\n"
      "v_dot2_f32_bf16 v140, v119, s70, v140\n"
      "v_dot2_f32_bf16 v141, v119, v119, v141\n"
      "s_waitcnt lgkmcnt(4)\n"
      "v_dot2_f32_bf16 v140, v122, s70, v140\n"
      "v_dot2_f32_bf16 v141, v122, v122, v141\n"
      "v_dot2_f32_bf16 v140, v123, s70, v140\n"
      "v_dot2_f32_bf16 v141, v123, v123, v141\n"
      "s_waitcnt lgkmcnt(3)\n"
      "v_dot2_f32_bf16 v140, v126, s70, v140\n"
      "v_dot2_f32_bf16 v141, v126, v126, v141\n"
      "v_dot2_f32_bf16 v140, v127, s70, v140\n"
      "v_dot2_f32_bf16 v141, v127, v127, v141\n"
      "s_waitcnt lgkmcnt(2)\n"
      "v_dot2_f32_bf16 v140, v130, s70, v140\n"
      "v_dot2_f32_bf16 v141, v130, v130, v141\n"
      "v_dot2_f32_bf16 v140, v131, s70, v140\n"
      "v_dot2_f32_bf16 v141, v131, v131, v141\n"
      "s_waitcnt lgkmcnt(1)\n"
      "v_dot2_f32_bf16 v140, v134, s70, v140\n"
      "v_dot2_f32_bf16 v141, v134, v134, v141\n"
      "v_dot2_f32_bf16 v140, v135, s70, v140\n"
      "v_dot2_f32_bf16 v141, v135, v135, v141\n"
      "s_waitcnt lgkmcnt(0)\n"
      "v_dot2_f32_bf16 v140, v138, s70, v140\n"
      "v_dot2_f32_bf16 v141, v138, v138, v141\n"
      "v_dot2_f32_bf16 v140, v139, s70, v140\n"
      "v_dot2_f32_bf16 v141, v139, v139, v141\n"
      "v_lshlrev_b32 v112, 16, v114\n"
      "v_and_b32 v113, s69, v114\n"
      "v_lshlrev_b32 v114, 16, v115\n"
      "v_and_b32 v115, s69, v115\n"
      "v_add_f32_dpp v140, v140, v140 row_ror:8 row_mask:0xf bank_mask:0xf\n"
      "v_add_f32_dpp v141, v141, v141 row_ror:8 row_mask:0xf bank_mask:0xf\n"
      "v_lshlrev_b32 v116, 16, v118\n"
      "v_and_b32 v117, s69, v118\n"
      "v_add_f32_dpp v140, v140, v140 row_ror:4 row_mask:0xf bank_mask:0xf\n"
      "v_add_f32_dpp v141, v141, v141 row_ror:4 row_mask:0xf bank_mask:0xf\n"
      "v_lshlrev_b32 v118, 16, v119\n"
      "v_and_b32 v119, s69, v119\n"
      "v_add_f32_dpp v140, v140, v140 row_ror:2 row_mask:0xf bank_mask:0xf\n"
      "v_add_f32_dpp v141, v141, v141 row_ror:2 row_mask:0xf bank_mask:0xf\n"
      "v_lshlrev_b32 v120, 16, v122\n"
      "v_and_b32 v121, s69, v122\n"
      "v_add_f32_dpp v140, v140, v140 row_ror:1 row_mask:0xf bank_mask:0xf\n"
      "v_add_f32_dpp v141, v141, v141 row_ror:1 row_mask:0xf bank_mask:0xf\n"
      "v_lshlrev_b32 v122, 16, v123\n"
      "v_and_b32 v123, s69, v123\n"
      "v_mul_f32 v140, 0x3b124925, v140\n"
      "v_lshlrev_b32 v124, 16, v126\n"
      "v_mul_f32 v141, 0x3b124925, v141\n"
      "v_and_b32 v125, s69, v126\n"
      "v_fma_f32 v141, -v140, v140, v141\n"
      "v_lshlrev_b32 v126, 16, v127\n"
      "v_max_f32 v141, 0, v141\n"
      "v_and_b32 v127, s69, v127\n"
      "v_add_f32 v141, 0x3727c5ac, v141\n"
      "v_lshlrev_b32 v128, 16, v130\n"
      "v_rsq_f32 v142, v141\n"
      "v_and_b32 v129, s69, v130\n"
      "v_lshlrev_b32 v130, 16, v131\n"
      "v_and_b32 v131, s69, v131\n"
      "v_lshlrev_b32 v132, 16, v134\n"
      "v_and_b32 v133, s69, v134\n"
      "v_lshlrev_b32 v134, 16, v135\n"
      "v_and_b32 v135, s69, v135\n"
      "v_lshlrev_b32 v136, 16, v138\n"
      "v_and_b32 v137, s69, v138\n"
      "v_lshlrev_b32 v138, 16, v139\n"
      "v_and_b32 v139, s69, v139\n"
      "v_mul_f32_e64 v143, -v140, v142\n"
      ".p2align 3\n"
      "v_pk_fma_f32 v[112:113], v[112:113], v[142:143], v[142:143] op_sel:[0,0,1] op_sel_hi:[1,0,1]\n"
      "v_pk_fma_f32 v[114:115], v[114:115], v[142:143], v[142:143] op_sel:[0,0,1] op_sel_hi:[1,0,1]\n"
      "v_pk_fma_f32 v[112:113], v[112:113], v[144:145], v[172:173]\n"
      "v_pk_fma_f32 v[114:115], v[114:115], v[146:147], v[174:175]\n"
      "v_cvt_pk_bf16_f32 v112, v112, v113\n"
      "v_cvt_pk_bf16_f32 v113, v114, v115\n"
      "v_pk_fma_f32 v[116:117], v[116:117], v[142:143], v[142:143] op_sel:[0,0,1] op_sel_hi:[1,0,1]\n"
      "v_pk_fma_f32 v[118:119], v[118:119], v[142:143], v[142:143] op_sel:[0,0,1] op_sel_hi:[1,0,1]\n"
      "v_pk_fma_f32 v[116:117], v[116:117], v[148:149], v[176:177]\n"
      "v_pk_fma_f32 v[118:119], v[118:119], v[150:151], v[178:179]\n"
      "v_cvt_pk_bf16_f32 v116, v116, v117\n"
      "v_cvt_pk_bf16_f32 v117, v118, v119\n"
      "ds_write_addtid_b32 v112 offset:57344\n"
      "ds_write_addtid_b32 v113 offset:57600\n"
      "v_pk_fma_f32 v[120:121], v[120:121], v[142:143], v[142:143] op_sel:[0,0,1] op_sel_hi:[1,0,1]\n"
      "v_pk_fma_f32 v[122:123], v[122:123], v[142:143], v[142:143] op_sel:[0,0,1] op_sel_hi:[1,0,1]\n"
      "v_pk_fma_f32 v[120:121], v[120:121], v[152:153], v[180:181]\n"
      "v_pk_fma_f32 v[122:123], v[122:123], v[154:155], v[182:183]\n"
      "v_cvt_pk_bf16_f32 v120, v120, v121\n"
      "v_cvt_pk_bf16_f32 v121, v122, v123\n"
      "ds_write_addtid_b32 v116 offset:57856\n"
      "ds_write_addtid_b32 v117 offset:58112\n"
      "v_pk_fma_f32 v[124:125], v[124:125], v[142:143], v[142:143] op_sel:[0,0,1] op_sel_hi:[1,0,1]\n"
      "v_pk_fma_f32 v[126:127], v[126:127], v[142:143], v[142:143] op_sel:[0,0,1] op_sel_hi:[1,0,1]\n"
      "v_pk_fma_f32 v[124:125], v[124:125], v[156:157], v[184:185]\n"
      "v_pk_fma_f32 v[126:127], v[126:127], v[158:159], v[186:187]\n"
      "v_cvt_pk_bf16_f32 v124, v124, v125\n"
      "v_cvt_pk_bf16_f32 v125, v126, v127\n"
      "ds_write_addtid_b32 v120 offset:58368\n"
      "ds_write_addtid_b32 v121 offset:58624\n"
      "v_pk_fma_f32 v[128:129], v[128:129], v[142:143], v[142:143] op_sel:[0,0,1] op_sel_hi:[1,0,1]\n"
      "v_pk_fma_f32 v[130:131], v[130:131], v[142:143], v[142:143] op_sel:[0,0,1] op_sel_hi:[1,0,1]\n"
      "v_pk_fma_f32 v[128:129], v[128:129], v[160:161], v[188:189]\n"
      "v_pk_fma_f32 v[130:131], v[130:131], v[162:163], v[190:191]\n"
      "v_cvt_pk_bf16_f32 v128, v128, v129\n"
      "v_cvt_pk_bf16_f32 v129, v130, v131\n"
      "ds_write_addtid_b32 v124 offset:58880\n"
      "ds_write_addtid_b32 v125 offset:59136\n"
      "v_pk_fma_f32 v[132:133], v[132:133], v[142:143], v[142:143] op_sel:[0,0,1] op_sel_hi:[1,0,1]\n"
      "v_pk_fma_f32 v[134:135], v[134:135], v[142:143], v[142:143] op_sel:[0,0,1] op_sel_hi:[1,0,1]\n"
      "v_pk_fma_f32 v[132:133], v[132:133], v[164:165], v[192:193]\n"
      "v_pk_fma_f32 v[134:135], v[134:135], v[166:167], v[194:195]\n"
      "v_cvt_pk_bf16_f32 v132, v132, v133\n"
      "v_cvt_pk_bf16_f32 v133, v134, v135\n"
      "ds_write_addtid_b32 v128 offset:59392\n"
      "ds_write_addtid_b32 v129 offset:59648\n"
      "v_pk_fma_f32 v[136:137], v[136:137], v[142:143], v[142:143] op_sel:[0,0,1] op_sel_hi:[1,0,1]\n"
      "v_pk_fma_f32 v[138:139], v[138:139], v[142:143], v[142:143] op_sel:[0,0,1] op_sel_hi:[1,0,1]\n"
      "v_pk_fma_f32 v[136:137], v[136:137], v[168:169], v[196:197]\n"
      "v_pk_fma_f32 v[138:139], v[138:139], v[170:171], v[198:199]\n"
      "v_cvt_pk_bf16_f32 v136, v136, v137\n"
      "v_cvt_pk_bf16_f32 v137, v138, v139\n"
      "ds_write_addtid_b32 v132 offset:59904\n"
      "ds_write_addtid_b32 v133 offset:60160\n"
      "ds_write_addtid_b32 v136 offset:60416\n"
      "ds_write_addtid_b32 v137 offset:60672\n"
      "s_waitcnt lgkmcnt(0)\n"
      "s_barrier\n"
      "ds_read_b64 v[114:115], v252 offset:3584\n"
      "ds_read_b64 v[118:119], v252 offset:3712\n"
      "ds_read_b64 v[122:123], v252 offset:3840\n"
      "ds_read_b64 v[126:127], v252 offset:3968\n"
      "ds_read_b64 v[130:131], v252 offset:4096\n"
      "ds_read_b64 v[134:135], v252 offset:4224\n"
      "ds_read_b64 v[138:139], v252 offset:4352\n"
      "s_waitcnt lgkmcnt(6)\n"
      "v_dot2_f32_bf16 v140, v114, s70, 0\n"
      "v_dot2_f32_bf16 v141, v114, v114, 0\n"
      "v_dot2_f32_bf16 v140, v115, s70, v140\n"
      "v_dot2_f32_bf16 v141, v115, v115, v141\n"
      "s_waitcnt lgkmcnt(5)\n"
      "v_dot2_f32_bf16 v140, v118, s70, v140\n"
      "v_dot2_f32_bf16 v141, v118, v118, v141\n"
      "v_dot2_f32_bf16 v140, v119, s70, v140\n"
      "v_dot2_f32_bf16 v141, v119, v119, v141\n"
      "s_waitcnt lgkmcnt(4)\n"
      "v_dot2_f32_bf16 v140, v122, s70, v140\n"
      "v_dot2_f32_bf16 v141, v122, v122, v141\n"
      "v_dot2_f32_bf16 v140, v123, s70, v140\n"
      "v_dot2_f32_bf16 v141, v123, v123, v141\n"
      "s_waitcnt lgkmcnt(3)\n"
      "v_dot2_f32_bf16 v140, v126, s70, v140\n"
      "v_dot2_f32_bf16 v141, v126, v126, v141\n"
      "v_dot2_f32_bf16 v140, v127, s70, v140\n"
      "v_dot2_f32_bf16 v141, v127, v127, v141\n"
      "s_waitcnt lgkmcnt(2)\n"
      "v_dot2_f32_bf16 v140, v130, s70, v140\n"
      "v_dot2_f32_bf16 v141, v130, v130, v141\n"
      "v_dot2_f32_bf16 v140, v131, s70, v140\n"
      "v_dot2_f32_bf16 v141, v131, v131, v141\n"
      "s_waitcnt lgkmcnt(1)\n"
      "v_dot2_f32_bf16 v140, v134, s70, v140\n"
      "v_dot2_f32_bf16 v141, v134, v134, v141\n"
      "v_dot2_f32_bf16 v140, v135, s70, v140\n"
      "v_dot2_f32_bf16 v141, v135, v135, v141\n"
      "s_waitcnt lgkmcnt(0)\n"
      "v_dot2_f32_bf16 v140, v138, s70, v140\n"
      "v_dot2_f32_bf16 v141, v138, v138, v141\n"
      "v_dot2_f32_bf16 v140, v139, s70, v140\n"
      "v_dot2_f32_bf16 v141, v139, v139, v141\n"
      "v_lshlrev_b32 v112, 16, v114\n"
      "v_and_b32 v113, s69, v114\n"
      "v_lshlrev_b32 v114, 16, v115\n"
      "v_and_b32 v115, s69, v115\n"
      "v_add_f32_dpp v140, v140, v140 row_ror:8 row_mask:0xf bank_mask:0xf\n"
      "v_add_f32_dpp v141, v141, v141 row_ror:8 row_mask:0xf bank_mask:0xf\n"
      "v_lshlrev_b32 v116, 16, v118\n"
      "v_and_b32 v117, s69, v118\n"
      "v_add_f32_dpp v140, v140, v140 row_ror:4 row_mask:0xf bank_mask:0xf\n"
      "v_add_f32_dpp v141, v141, v141 row_ror:4 row_mask:0xf bank_mask:0xf\n"
      "v_lshlrev_b32 v118, 16, v119\n"
      "v_and_b32 v119, s69, v119\n"
      "v_add_f32_dpp v140, v140, v140 row_ror:2 row_mask:0xf bank_mask:0xf\n"
      "v_add_f32_dpp v141, v141, v141 row_ror:2 row_mask:0xf bank_mask:0xf\n"
      "v_lshlrev_b32 v120, 16, v122\n"
      "v_and_b32 v121, s69, v122\n"
      "v_add_f32_dpp v140, v140, v140 row_ror:1 row_mask:0xf bank_mask:0xf\n"
      "v_add_f32_dpp v141, v141, v141 row_ror:1 row_mask:0xf bank_mask:0xf\n"
      "v_lshlrev_b32 v122, 16, v123\n"
      "v_and_b32 v123, s69, v123\n"
      "v_mul_f32 v140, 0x3b124925, v140\n"
      "v_lshlrev_b32 v124, 16, v126\n"
      "v_mul_f32 v141, 0x3b124925, v141\n"
      "v_and_b32 v125, s69, v126\n"
      "v_fma_f32 v141, -v140, v140, v141\n"
      "v_lshlrev_b32 v126, 16, v127\n"
      "v_max_f32 v141, 0, v141\n"
      "v_and_b32 v127, s69, v127\n"
      "v_add_f32 v141, 0x3727c5ac, v141\n"
      "v_lshlrev_b32 v128, 16, v130\n"
      "v_rsq_f32 v142, v141\n"
      "v_and_b32 v129, s69, v130\n"
      "v_lshlrev_b32 v130, 16, v131\n"
      "v_and_b32 v131, s69, v131\n"
      "v_lshlrev_b32 v132, 16, v134\n"
      "v_and_b32 v133, s69, v134\n"
      "v_lshlrev_b32 v134, 16, v135\n"
      "v_and_b32 v135, s69, v135\n"
      "v_lshlrev_b32 v136, 16, v138\n"
      "v_and_b32 v137, s69, v138\n"
      "v_lshlrev_b32 v138, 16, v139\n"
      "v_and_b32 v139, s69, v139\n"
      "v_mul_f32_e64 v143, -v140, v142\n"
      ".p2align 3\n"
      "v_pk_fma_f32 v[112:113], v[112:113], v[142:143], v[142:143] op_sel:[0,0,1] op_sel_hi:[1,0,1]\n"
      "v_pk_fma_f32 v[114:115], v[114:115], v[142:143], v[142:143] op_sel:[0,0,1] op_sel_hi:[1,0,1]\n"
      "v_pk_fma_f32 v[112:113], v[112:113], v[144:145], v[172:173]\n"
      "v_pk_fma_f32 v[114:115], v[114:115], v[146:147], v[174:175]\n"
      "v_cvt_pk_bf16_f32 v112, v112, v113\n"
      "v_cvt_pk_bf16_f32 v113, v114, v115\n"
      "v_pk_fma_f32 v[116:117], v[116:117], v[142:143], v[142:143] op_sel:[0,0,1] op_sel_hi:[1,0,1]\n"
      "v_pk_fma_f32 v[118:119], v[118:119], v[142:143], v[142:143] op_sel:[0,0,1] op_sel_hi:[1,0,1]\n"
      "v_pk_fma_f32 v[116:117], v[116:117], v[148:149], v[176:177]\n"
      "v_pk_fma_f32 v[118:119], v[118:119], v[150:151], v[178:179]\n"
      "v_cvt_pk_bf16_f32 v116, v116, v117\n"
      "v_cvt_pk_bf16_f32 v117, v118, v119\n"
      "ds_write_addtid_b32 v112 offset:60928\n"
      "ds_write_addtid_b32 v113 offset:61184\n"
      "v_pk_fma_f32 v[120:121], v[120:121], v[142:143], v[142:143] op_sel:[0,0,1] op_sel_hi:[1,0,1]\n"
      "v_pk_fma_f32 v[122:123], v[122:123], v[142:143], v[142:143] op_sel:[0,0,1] op_sel_hi:[1,0,1]\n"
      "v_pk_fma_f32 v[120:121], v[120:121], v[152:153], v[180:181]\n"
      "v_pk_fma_f32 v[122:123], v[122:123], v[154:155], v[182:183]\n"
      "v_cvt_pk_bf16_f32 v120, v120, v121\n"
      "v_cvt_pk_bf16_f32 v121, v122, v123\n"
      "ds_write_addtid_b32 v116 offset:61440\n"
      "ds_write_addtid_b32 v117 offset:61696\n"
      "v_pk_fma_f32 v[124:125], v[124:125], v[142:143], v[142:143] op_sel:[0,0,1] op_sel_hi:[1,0,1]\n"
      "v_pk_fma_f32 v[126:127], v[126:127], v[142:143], v[142:143] op_sel:[0,0,1] op_sel_hi:[1,0,1]\n"
      "v_pk_fma_f32 v[124:125], v[124:125], v[156:157], v[184:185]\n"
      "v_pk_fma_f32 v[126:127], v[126:127], v[158:159], v[186:187]\n"
      "v_cvt_pk_bf16_f32 v124, v124, v125\n"
      "v_cvt_pk_bf16_f32 v125, v126, v127\n"
      "ds_write_addtid_b32 v120 offset:61952\n"
      "ds_write_addtid_b32 v121 offset:62208\n"
      "v_pk_fma_f32 v[128:129], v[128:129], v[142:143], v[142:143] op_sel:[0,0,1] op_sel_hi:[1,0,1]\n"
      "v_pk_fma_f32 v[130:131], v[130:131], v[142:143], v[142:143] op_sel:[0,0,1] op_sel_hi:[1,0,1]\n"
      "v_pk_fma_f32 v[128:129], v[128:129], v[160:161], v[188:189]\n"
      "v_pk_fma_f32 v[130:131], v[130:131], v[162:163], v[190:191]\n"
      "v_cvt_pk_bf16_f32 v128, v128, v129\n"
      "v_cvt_pk_bf16_f32 v129, v130, v131\n"
      "ds_write_addtid_b32 v124 offset:62464\n"
      "ds_write_addtid_b32 v125 offset:62720\n"
      "v_pk_fma_f32 v[132:133], v[132:133], v[142:143], v[142:143] op_sel:[0,0,1] op_sel_hi:[1,0,1]\n"
      "v_pk_fma_f32 v[134:135], v[134:135], v[142:143], v[142:143] op_sel:[0,0,1] op_sel_hi:[1,0,1]\n"
      "v_pk_fma_f32 v[132:133], v[132:133], v[164:165], v[192:193]\n"
      "v_pk_fma_f32 v[134:135], v[134:135], v[166:167], v[194:195]\n"
      "v_cvt_pk_bf16_f32 v132, v132, v133\n"
      "v_cvt_pk_bf16_f32 v133, v134, v135\n"
      "ds_write_addtid_b32 v128 offset:62976\n"
      "ds_write_addtid_b32 v129 offset:63232\n"
      "v_pk_fma_f32 v[136:137], v[136:137], v[142:143], v[142:143] op_sel:[0,0,1] op_sel_hi:[1,0,1]\n"
      "v_pk_fma_f32 v[138:139], v[138:139], v[142:143], v[142:143] op_sel:[0,0,1] op_sel_hi:[1,0,1]\n"
      "v_pk_fma_f32 v[136:137], v[136:137], v[168:169], v[196:197]\n"
      "v_pk_fma_f32 v[138:139], v[138:139], v[170:171], v[198:199]\n"
      "v_cvt_pk_bf16_f32 v136, v136, v137\n"
      "v_cvt_pk_bf16_f32 v137, v138, v139\n"
      "ds_write_addtid_b32 v132 offset:63488\n"
      "ds_write_addtid_b32 v133 offset:63744\n"
      "ds_write_addtid_b32 v136 offset:64000\n"
      "ds_write_addtid_b32 v137 offset:64256\n"
      "s_waitcnt lgkmcnt(0)\n"
      "s_barrier\n"
      "ds_read_b64 v[114:115], v252 offset:28672\n"
      "ds_read_b64 v[118:119], v252 offset:28800\n"
      "ds_read_b64 v[122:123], v252 offset:28928\n"
      "ds_read_b64 v[126:127], v252 offset:29056\n"
      "ds_read_b64 v[130:131], v252 offset:29184\n"
      "ds_read_b64 v[134:135], v252 offset:29312\n"
      "ds_read_b64 v[138:139], v252 offset:29440\n"
      "s_waitcnt lgkmcnt(6)\n"
      "v_dot2_f32_bf16 v140, v114, s70, 0\n"
      "v_dot2_f32_bf16 v141, v114, v114, 0\n"
      "v_dot2_f32_bf16 v140, v115, s70, v140\n"
      "v_dot2_f32_bf16 v141, v115, v115, v141\n"
      "s_waitcnt lgkmcnt(5)\n"
      "v_dot2_f32_bf16 v140, v118, s70, v140\n"
      "v_dot2_f32_bf16 v141, v118, v118, v141\n"
      "v_dot2_f32_bf16 v140, v119, s70, v140\n"
      "v_dot2_f32_bf16 v141, v119, v119, v141\n"
      "s_waitcnt lgkmcnt(4)\n"
      "v_dot2_f32_bf16 v140, v122, s70, v140\n"
      "v_dot2_f32_bf16 v141, v122, v122, v141\n"
      "v_dot2_f32_bf16 v140, v123, s70, v140\n"
      "v_dot2_f32_bf16 v141, v123, v123, v141\n"
      "s_waitcnt lgkmcnt(3)\n"
      "v_dot2_f32_bf16 v140, v126, s70, v140\n"
      "v_dot2_f32_bf16 v141, v126, v126, v141\n"
      "v_dot2_f32_bf16 v140, v127, s70, v140\n"
      "v_dot2_f32_bf16 v141, v127, v127, v141\n"
      "s_waitcnt lgkmcnt(2)\n"
      "v_dot2_f32_bf16 v140, v130, s70, v140\n"
      "v_dot2_f32_bf16 v141, v130, v130, v141\n"
      "v_dot2_f32_bf16 v140, v131, s70, v140\n"
      "v_dot2_f32_bf16 v141, v131, v131, v141\n"
      "s_waitcnt lgkmcnt(1)\n"
      "v_dot2_f32_bf16 v140, v134, s70, v140\n"
      "v_dot2_f32_bf16 v141, v134, v134, v141\n"
      "v_dot2_f32_bf16 v140, v135, s70, v140\n"
      "v_dot2_f32_bf16 v141, v135, v135, v141\n"
      "s_waitcnt lgkmcnt(0)\n"
      "v_dot2_f32_bf16 v140, v138, s70, v140\n"
      "v_dot2_f32_bf16 v141, v138, v138, v141\n"
      "v_dot2_f32_bf16 v140, v139, s70, v140\n"
      "v_dot2_f32_bf16 v141, v139, v139, v141\n"
      "v_lshlrev_b32 v112, 16, v114\n"
      "v_and_b32 v113, s69, v114\n"
      "v_lshlrev_b32 v114, 16, v115\n"
      "v_and_b32 v115, s69, v115\n"
      "v_add_f32_dpp v140, v140, v140 row_ror:8 row_mask:0xf bank_mask:0xf\n"
      "v_add_f32_dpp v141, v141, v141 row_ror:8 row_mask:0xf bank_mask:0xf\n"
      "v_lshlrev_b32 v116, 16, v118\n"
      "v_and_b32 v117, s69, v118\n"
      "v_add_f32_dpp v140, v140, v140 row_ror:4 row_mask:0xf bank_mask:0xf\n"
      "v_add_f32_dpp v141, v141, v141 row_ror:4 row_mask:0xf bank_mask:0xf\n"
      "v_lshlrev_b32 v118, 16, v119\n"
      "v_and_b32 v119, s69, v119\n"
      "v_add_f32_dpp v140, v140, v140 row_ror:2 row_mask:0xf bank_mask:0xf\n"
      "v_add_f32_dpp v141, v141, v141 row_ror:2 row_mask:0xf bank_mask:0xf\n"
      "v_lshlrev_b32 v120, 16, v122\n"
      "v_and_b32 v121, s69, v122\n"
      "v_add_f32_dpp v140, v140, v140 row_ror:1 row_mask:0xf bank_mask:0xf\n"
      "v_add_f32_dpp v141, v141, v141 row_ror:1 row_mask:0xf bank_mask:0xf\n"
      "v_lshlrev_b32 v122, 16, v123\n"
      "v_and_b32 v123, s69, v123\n"
      "v_mul_f32 v140, 0x3b124925, v140\n"
      "v_lshlrev_b32 v124, 16, v126\n"
      "v_mul_f32 v141, 0x3b124925, v141\n"
      "v_and_b32 v125, s69, v126\n"
      "v_fma_f32 v141, -v140, v140, v141\n"
      "v_lshlrev_b32 v126, 16, v127\n"
      "v_max_f32 v141, 0, v141\n"
      "v_and_b32 v127, s69, v127\n"
      "v_add_f32 v141, 0x3727c5ac, v141\n"
      "v_lshlrev_b32 v128, 16, v130\n"
      "v_rsq_f32 v142, v141\n"
      "v_and_b32 v129, s69, v130\n"
      "v_lshlrev_b32 v130, 16, v131\n"
      "v_and_b32 v131, s69, v131\n"
      "v_lshlrev_b32 v132, 16, v134\n"
      "v_and_b32 v133, s69, v134\n"
      "v_lshlrev_b32 v134, 16, v135\n"
      "v_and_b32 v135, s69, v135\n"
      "v_lshlrev_b32 v136, 16, v138\n"
      "v_and_b32 v137, s69, v138\n"
      "v_lshlrev_b32 v138, 16, v139\n"
      "v_and_b32 v139, s69, v139\n"
      "v_mul_f32_e64 v143, -v140, v142\n"
      ".p2align 3\n"
      "v_pk_fma_f32 v[112:113], v[112:113], v[142:143], v[142:143] op_sel:[0,0,1] op_sel_hi:[1,0,1]\n"
      "v_pk_fma_f32 v[114:115], v[114:115], v[142:143], v[142:143] op_sel:[0,0,1] op_sel_hi:[1,0,1]\n"
      "v_pk_fma_f32 v[112:113], v[112:113], v[144:145], v[172:173]\n"
      "v_pk_fma_f32 v[114:115], v[114:115], v[146:147], v[174:175]\n"
      "v_cvt_pk_bf16_f32 v112, v112, v113\n"
      "v_cvt_pk_bf16_f32 v113, v114, v115\n"
      "v_pk_fma_f32 v[116:117], v[116:117], v[142:143], v[142:143] op_sel:[0,0,1] op_sel_hi:[1,0,1]\n"
      "v_pk_fma_f32 v[118:119], v[118:119], v[142:143], v[142:143] op_sel:[0,0,1] op_sel_hi:[1,0,1]\n"
      "v_pk_fma_f32 v[116:117], v[116:117], v[148:149], v[176:177]\n"
      "v_pk_fma_f32 v[118:119], v[118:119], v[150:151], v[178:179]\n"
      "v_cvt_pk_bf16_f32 v116, v116, v117\n"
      "v_cvt_pk_bf16_f32 v117, v118, v119\n"
      "s_addk_i32 m0, 28672\n"
      "s_nop 0\n"
      "ds_write_addtid_b32 v112 offset:57344\n"
      "ds_write_addtid_b32 v113 offset:57600\n"
      "v_pk_fma_f32 v[120:121], v[120:121], v[142:143], v[142:143] op_sel:[0,0,1] op_sel_hi:[1,0,1]\n"
      "v_pk_fma_f32 v[122:123], v[122:123], v[142:143], v[142:143] op_sel:[0,0,1] op_sel_hi:[1,0,1]\n"
      "v_pk_fma_f32 v[120:121], v[120:121], v[152:153], v[180:181]\n"
      "v_pk_fma_f32 v[122:123], v[122:123], v[154:155], v[182:183]\n"
      "v_cvt_pk_bf16_f32 v120, v120, v121\n"
      "v_cvt_pk_bf16_f32 v121, v122, v123\n"
      "ds_write_addtid_b32 v116 offset:57856\n"
      "ds_write_addtid_b32 v117 offset:58112\n"
      "v_pk_fma_f32 v[124:125], v[124:125], v[142:143], v[142:143] op_sel:[0,0,1] op_sel_hi:[1,0,1]\n"
      "v_pk_fma_f32 v[126:127], v[126:127], v[142:143], v[142:143] op_sel:[0,0,1] op_sel_hi:[1,0,1]\n"
      "v_pk_fma_f32 v[124:125], v[124:125], v[156:157], v[184:185]\n"
      "v_pk_fma_f32 v[126:127], v[126:127], v[158:159], v[186:187]\n"
      "v_cvt_pk_bf16_f32 v124, v124, v125\n"
      "v_cvt_pk_bf16_f32 v125, v126, v127\n"
      "ds_write_addtid_b32 v120 offset:58368\n"
      "ds_write_addtid_b32 v121 offset:58624\n"
      "v_pk_fma_f32 v[128:129], v[128:129], v[142:143], v[142:143] op_sel:[0,0,1] op_sel_hi:[1,0,1]\n"
      "v_pk_fma_f32 v[130:131], v[130:131], v[142:143], v[142:143] op_sel:[0,0,1] op_sel_hi:[1,0,1]\n"
      "v_pk_fma_f32 v[128:129], v[128:129], v[160:161], v[188:189]\n"
      "v_pk_fma_f32 v[130:131], v[130:131], v[162:163], v[190:191]\n"
      "v_cvt_pk_bf16_f32 v128, v128, v129\n"
      "v_cvt_pk_bf16_f32 v129, v130, v131\n"
      "ds_write_addtid_b32 v124 offset:58880\n"
      "ds_write_addtid_b32 v125 offset:59136\n"
      "v_pk_fma_f32 v[132:133], v[132:133], v[142:143], v[142:143] op_sel:[0,0,1] op_sel_hi:[1,0,1]\n"
      "v_pk_fma_f32 v[134:135], v[134:135], v[142:143], v[142:143] op_sel:[0,0,1] op_sel_hi:[1,0,1]\n"
      "v_pk_fma_f32 v[132:133], v[132:133], v[164:165], v[192:193]\n"
      "v_pk_fma_f32 v[134:135], v[134:135], v[166:167], v[194:195]\n"
      "v_cvt_pk_bf16_f32 v132, v132, v133\n"
      "v_cvt_pk_bf16_f32 v133, v134, v135\n"
      "ds_write_addtid_b32 v128 offset:59392\n"
      "ds_write_addtid_b32 v129 offset:59648\n"
      "v_pk_fma_f32 v[136:137], v[136:137], v[142:143], v[142:143] op_sel:[0,0,1] op_sel_hi:[1,0,1]\n"
      "v_pk_fma_f32 v[138:139], v[138:139], v[142:143], v[142:143] op_sel:[0,0,1] op_sel_hi:[1,0,1]\n"
      "v_pk_fma_f32 v[136:137], v[136:137], v[168:169], v[196:197]\n"
      "v_pk_fma_f32 v[138:139], v[138:139], v[170:171], v[198:199]\n"
      "v_cvt_pk_bf16_f32 v136, v136, v137\n"
      "v_cvt_pk_bf16_f32 v137, v138, v139\n"
      "ds_write_addtid_b32 v132 offset:59904\n"
      "ds_write_addtid_b32 v133 offset:60160\n"
      "ds_write_addtid_b32 v136 offset:60416\n"
      "ds_write_addtid_b32 v137 offset:60672\n"
      "s_waitcnt lgkmcnt(0)\n"
      "s_barrier\n"
      "ds_read_b64 v[114:115], v252 offset:32256\n"
      "ds_read_b64 v[118:119], v252 offset:32384\n"
      "ds_read_b64 v[122:123], v252 offset:32512\n"
      "ds_read_b64 v[126:127], v252 offset:32640\n"
      "ds_read_b64 v[130:131], v252 offset:32768\n"
      "ds_read_b64 v[134:135], v252 offset:32896\n"
      "ds_read_b64 v[138:139], v252 offset:33024\n"
      "s_waitcnt lgkmcnt(6)\n"
      "v_dot2_f32_bf16 v140, v114, s70, 0\n"
      "v_dot2_f32_bf16 v141, v114, v114, 0\n"
      "v_dot2_f32_bf16 v140, v115, s70, v140\n"
      "v_dot2_f32_bf16 v141, v115, v115, v141\n"
      "s_waitcnt lgkmcnt(5)\n"
      "v_dot2_f32_bf16 v140, v118, s70, v140\n"
      "v_dot2_f32_bf16 v141, v118, v118, v141\n"
      "v_dot2_f32_bf16 v140, v119, s70, v140\n"
      "v_dot2_f32_bf16 v141, v119, v119, v141\n"
      "s_waitcnt lgkmcnt(4)\n"
      "v_dot2_f32_bf16 v140, v122, s70, v140\n"
      "v_dot2_f32_bf16 v141, v122, v122, v141\n"
      "v_dot2_f32_bf16 v140, v123, s70, v140\n"
      "v_dot2_f32_bf16 v141, v123, v123, v141\n"
      "s_waitcnt lgkmcnt(3)\n"
      "v_dot2_f32_bf16 v140, v126, s70, v140\n"
      "v_dot2_f32_bf16 v141, v126, v126, v141\n"
      "v_dot2_f32_bf16 v140, v127, s70, v140\n"
      "v_dot2_f32_bf16 v141, v127, v127, v141\n"
      "s_waitcnt lgkmcnt(2)\n"
      "v_dot2_f32_bf16 v140, v130, s70, v140\n"
      "v_dot2_f32_bf16 v141, v130, v130, v141\n"
      "v_dot2_f32_bf16 v140, v131, s70, v140\n"
      "v_dot2_f32_bf16 v141, v131, v131, v141\n"
      "s_waitcnt lgkmcnt(1)\n"
      "v_dot2_f32_bf16 v140, v134, s70, v140\n"
      "v_dot2_f32_bf16 v141, v134, v134, v141\n"
      "v_dot2_f32_bf16 v140, v135, s70, v140\n"
      "v_dot2_f32_bf16 v141, v135, v135, v141\n"
      "s_waitcnt lgkmcnt(0)\n"
      "v_dot2_f32_bf16 v140, v138, s70, v140\n"
      "v_dot2_f32_bf16 v141, v138, v138, v141\n"
      "v_dot2_f32_bf16 v140, v139, s70, v140\n"
      "v_dot2_f32_bf16 v141, v139, v139, v141\n"
      "v_lshlrev_b32 v112, 16, v114\n"
      "v_and_b32 v113, s69, v114\n"
      "v_lshlrev_b32 v114, 16, v115\n"
      "v_and_b32 v115, s69, v115\n"
      "v_add_f32_dpp v140, v140, v140 row_ror:8 row_mask:0xf bank_mask:0xf\n"
      "v_add_f32_dpp v141, v141, v141 row_ror:8 row_mask:0xf bank_mask:0xf\n"
      "v_lshlrev_b32 v116, 16, v118\n"
      "v_and_b32 v117, s69, v118\n"
      "v_add_f32_dpp v140, v140, v140 row_ror:4 row_mask:0xf bank_mask:0xf\n"
      "v_add_f32_dpp v141, v141, v141 row_ror:4 row_mask:0xf bank_mask:0xf\n"
      "v_lshlrev_b32 v118, 16, v119\n"
      "v_and_b32 v119, s69, v119\n"
      "v_add_f32_dpp v140, v140, v140 row_ror:2 row_mask:0xf bank_mask:0xf\n"
      "v_add_f32_dpp v141, v141, v141 row_ror:2 row_mask:0xf bank_mask:0xf\n"
      "v_lshlrev_b32 v120, 16, v122\n"
      "v_and_b32 v121, s69, v122\n"
      "v_add_f32_dpp v140, v140, v140 row_ror:1 row_mask:0xf bank_mask:0xf\n"
      "v_add_f32_dpp v141, v141, v141 row_ror:1 row_mask:0xf bank_mask:0xf\n"
      "v_lshlrev_b32 v122, 16, v123\n"
      "v_and_b32 v123, s69, v123\n"
      "v_mul_f32 v140, 0x3b124925, v140\n"
      "v_lshlrev_b32 v124, 16, v126\n"
      "v_mul_f32 v141, 0x3b124925, v141\n"
      "v_and_b32 v125, s69, v126\n"
      "v_fma_f32 v141, -v140, v140, v141\n"
      "v_lshlrev_b32 v126, 16, v127\n"
      "v_max_f32 v141, 0, v141\n"
      "v_and_b32 v127, s69, v127\n"
      "v_add_f32 v141, 0x3727c5ac, v141\n"
      "v_lshlrev_b32 v128, 16, v130\n"
      "v_rsq_f32 v142, v141\n"
      "v_and_b32 v129, s69, v130\n"
      "v_lshlrev_b32 v130, 16, v131\n"
      "v_and_b32 v131, s69, v131\n"
      "v_lshlrev_b32 v132, 16, v134\n"
      "v_and_b32 v133, s69, v134\n"
      "v_lshlrev_b32 v134, 16, v135\n"
      "v_and_b32 v135, s69, v135\n"
      "v_lshlrev_b32 v136, 16, v138\n"
      "v_and_b32 v137, s69, v138\n"
      "v_lshlrev_b32 v138, 16, v139\n"
      "v_and_b32 v139, s69, v139\n"
      "v_mul_f32_e64 v143, -v140, v142\n"
      ".p2align 3\n"
      "v_pk_fma_f32 v[112:113], v[112:113], v[142:143], v[142:143] op_sel:[0,0,1] op_sel_hi:[1,0,1]\n"
      "v_pk_fma_f32 v[114:115], v[114:115], v[142:143], v[142:143] op_sel:[0,0,1] op_sel_hi:[1,0,1]\n"
      "v_pk_fma_f32 v[112:113], v[112:113], v[144:145], v[172:173]\n"
      "v_pk_fma_f32 v[114:115], v[114:115], v[146:147], v[174:175]\n"
      "v_cvt_pk_bf16_f32 v112, v112, v113\n"
      "v_cvt_pk_bf16_f32 v113, v114, v115\n"
      "v_pk_fma_f32 v[116:117], v[116:117], v[142:143], v[142:143] op_sel:[0,0,1] op_sel_hi:[1,0,1]\n"
      "v_pk_fma_f32 v[118:119], v[118:119], v[142:143], v[142:143] op_sel:[0,0,1] op_sel_hi:[1,0,1]\n"
      "v_pk_fma_f32 v[116:117], v[116:117], v[148:149], v[176:177]\n"
      "v_pk_fma_f32 v[118:119], v[118:119], v[150:151], v[178:179]\n"
      "v_cvt_pk_bf16_f32 v116, v116, v117\n"
      "v_cvt_pk_bf16_f32 v117, v118, v119\n"
      "ds_write_addtid_b32 v112 offset:60928\n"
      "ds_write_addtid_b32 v113 offset:61184\n"
      "v_pk_fma_f32 v[120:121], v[120:121], v[142:143], v[142:143] op_sel:[0,0,1] op_sel_hi:[1,0,1]\n"
      "v_pk_fma_f32 v[122:123], v[122:123], v[142:143], v[142:143] op_sel:[0,0,1] op_sel_hi:[1,0,1]\n"
      "v_pk_fma_f32 v[120:121], v[120:121], v[152:153], v[180:181]\n"
      "v_pk_fma_f32 v[122:123], v[122:123], v[154:155], v[182:183]\n"
      "v_cvt_pk_bf16_f32 v120, v120, v121\n"
      "v_cvt_pk_bf16_f32 v121, v122, v123\n"
      "ds_write_addtid_b32 v116 offset:61440\n"
      "ds_write_addtid_b32 v117 offset:61696\n"
      "v_pk_fma_f32 v[124:125], v[124:125], v[142:143], v[142:143] op_sel:[0,0,1] op_sel_hi:[1,0,1]\n"
      "v_pk_fma_f32 v[126:127], v[126:127], v[142:143], v[142:143] op_sel:[0,0,1] op_sel_hi:[1,0,1]\n"
      "v_pk_fma_f32 v[124:125], v[124:125], v[156:157], v[184:185]\n"
      "v_pk_fma_f32 v[126:127], v[126:127], v[158:159], v[186:187]\n"
      "v_cvt_pk_bf16_f32 v124, v124, v125\n"
      "v_cvt_pk_bf16_f32 v125, v126, v127\n"
      "ds_write_addtid_b32 v120 offset:61952\n"
      "ds_write_addtid_b32 v121 offset:62208\n"
      "v_pk_fma_f32 v[128:129], v[128:129], v[142:143], v[142:143] op_sel:[0,0,1] op_sel_hi:[1,0,1]\n"
      "v_pk_fma_f32 v[130:131], v[130:131], v[142:143], v[142:143] op_sel:[0,0,1] op_sel_hi:[1,0,1]\n"
      "v_pk_fma_f32 v[128:129], v[128:129], v[160:161], v[188:189]\n"
      "v_pk_fma_f32 v[130:131], v[130:131], v[162:163], v[190:191]\n"
      "v_cvt_pk_bf16_f32 v128, v128, v129\n"
      "v_cvt_pk_bf16_f32 v129, v130, v131\n"
      "ds_write_addtid_b32 v124 offset:62464\n"
      "ds_write_addtid_b32 v125 offset:62720\n"
      "v_pk_fma_f32 v[132:133], v[132:133], v[142:143], v[142:143] op_sel:[0,0,1] op_sel_hi:[1,0,1]\n"
      "v_pk_fma_f32 v[134:135], v[134:135], v[142:143], v[142:143] op_sel:[0,0,1] op_sel_hi:[1,0,1]\n"
      "v_pk_fma_f32 v[132:133], v[132:133], v[164:165], v[192:193]\n"
      "v_pk_fma_f32 v[134:135], v[134:135], v[166:167], v[194:195]\n"
      "v_cvt_pk_bf16_f32 v132, v132, v133\n"
      "v_cvt_pk_bf16_f32 v133, v134, v135\n"
      "ds_write_addtid_b32 v128 offset:62976\n"
      "ds_write_addtid_b32 v129 offset:63232\n"
      "v_pk_fma_f32 v[136:137], v[136:137], v[142:143], v[142:143] op_sel:[0,0,1] op_sel_hi:[1,0,1]\n"
      "v_pk_fma_f32 v[138:139], v[138:139], v[142:143], v[142:143] op_sel:[0,0,1] op_sel_hi:[1,0,1]\n"
      "v_pk_fma_f32 v[136:137], v[136:137], v[168:169], v[196:197]\n"
      "v_pk_fma_f32 v[138:139], v[138:139], v[170:171], v[198:199]\n"
      "v_cvt_pk_bf16_f32 v136, v136, v137\n"
      "v_cvt_pk_bf16_f32 v137, v138, v139\n"
      "ds_write_addtid_b32 v132 offset:63488\n"
      "ds_write_addtid_b32 v133 offset:63744\n"
      "ds_write_addtid_b32 v136 offset:64000\n"
      "ds_write_addtid_b32 v137 offset:64256\n"
      "s_waitcnt lgkmcnt(0)\n"
      "s_barrier\n"
      "s_setprio 2\n"
      "s_add_u32 m0, s66, 20480\n"
      "s_mov_b32 s67, 344064\n"
      "buffer_load_dwordx4 v244, s[44:47], s67 offen lds\n"
      "s_addk_i32 m0, 4096\n"
      "s_addk_i32 s67, 1792\n"
      "buffer_load_dwordx4 v244, s[44:47], s67 offen lds\n"
      "s_waitcnt vmcnt(0)\n"
      "s_barrier\n"
      "s_addk_i32 m0, 4096\n"
      "s_movk_i32 s67, 128\n"
      "buffer_load_dwordx4 v244, s[44:47], s67 offen lds\n"
      "s_addk_i32 m0, 4096\n"
      "s_movk_i32 s67, 1920\n"
      "buffer_load_dwordx4 v244, s[44:47], s67 offen lds\n"
      "s_addk_i32 m0, 4096\n"
      "s_mov_b32 s67, 57472\n"
      "buffer_load_dwordx4 v244, s[44:47], s67 offen lds\n"
      "s_addk_i32 m0, 4096\n"
      "s_addk_i32 s67, 1792\n"
      "buffer_load_dwordx4 v244, s[44:47], s67 offen lds\n"
      "s_addk_i32 m0, 4096\n"
      "s_mov_b32 s67, 114816\n"
      "buffer_load_dwordx4 v244, s[44:47], s67 offen lds\n"
      "s_addk_i32 m0, 4096\n"
      "s_addk_i32 s67, 1792\n"
      "buffer_load_dwordx4 v244, s[44:47], s67 offen lds\n"
      "s_addk_i32 m0, 4096\n"
      "s_mov_b32 s67, 172160\n"
      "buffer_load_dwordx4 v244, s[44:47], s67 offen lds\n"
      "s_waitcnt vmcnt(0)\n"
      "s_barrier\n"
      "s_add_u32 m0, s66, 92160\n"
      "s_addk_i32 s67, 1792\n"
      "buffer_load_dwordx4 v244, s[44:47], s67 offen lds\n"
      "s_addk_i32 m0, 4096\n"
      "s_mov_b32 s67, 229504\n"
      "buffer_load_dwordx4 v244, s[44:47], s67 offen lds\n"
      "s_addk_i32 m0, 4096\n"
      "s_addk_i32 s67, 1792\n"
      "buffer_load_dwordx4 v244, s[44:47], s67 offen lds\n"
      "s_addk_i32 m0, 4096\n"
      "s_mov_b32 s67, 286848\n"
      "buffer_load_dwordx4 v244, s[44:47], s67 offen lds\n"
      "s_addk_i32 m0, 4096\n"
      "s_addk_i32 s67, 1792\n"
      "buffer_load_dwordx4 v244, s[44:47], s67 offen lds\n"
      "s_addk_i32 m0, 4096\n"
      "s_mov_b32 s67, 344192\n"
      "buffer_load_dwordx4 v244, s[44:47], s67 offen lds\n"
      "s_addk_i32 m0, 4096\n"
      "s_addk_i32 s67, 1792\n"
      "buffer_load_dwordx4 v244, s[44:47], s67 offen lds\n"
      "s_waitcnt vmcnt(0)\n"
      "s_barrier\n"
      "s_addk_i32 m0, 4096\n"
      "s_movk_i32 s67, 256\n"
      "buffer_load_dwordx4 v244, s[44:47], s67 offen lds\n"
      "s_addk_i32 m0, 4096\n"
      "s_movk_i32 s67, 2048\n"
      "buffer_load_dwordx4 v244, s[44:47], s67 offen lds\n"
      "s_addk_i32 m0, 4096\n"
      "s_mov_b32 s67, 57600\n"
      "buffer_load_dwordx4 v244, s[44:47], s67 offen lds\n"
      "s_addk_i32 m0, 4096\n"
      "s_addk_i32 s67, 1792\n"
      "buffer_load_dwordx4 v244, s[44:47], s67 offen lds\n"
      "s_addk_i32 m0, 4096\n"
      "s_mov_b32 s67, 114944\n"
      "buffer_load_dwordx4 v244, s[44:47], s67 offen lds\n"
      "s_addk_i32 m0, 4096\n"
      "s_addk_i32 s67, 1792\n"
      "buffer_load_dwordx4 v244, s[44:47], s67 offen lds\n"
      "s_addk_i32 m0, 4096\n"
      "s_mov_b32 s67, 172288\n"
      "buffer_load_dwordx4 v244, s[44:47], s67 offen lds\n"
      "ds_read2_b32 v[0:1], v246 offset0:0 offset1:66\n"
      "ds_read2_b32 v[2:3], v246 offset0:16 offset1:82\n"
      "ds_read2_b32 v[4:5], v246 offset0:8 offset1:74\n"
      "ds_read2_b32 v[6:7], v246 offset0:24 offset1:90\n"
      "s_movk_i32 s68, 0\n"
      "s_waitcnt lgkmcnt(2)\n"
      "buffer_store_dwordx4 v[0:3], v245, s[48:51], s68 offen sc1\n"
      "s_movk_i32 s68, 14336\n"
      "s_waitcnt lgkmcnt(0)\n"
      "buffer_store_dwordx4 v[4:7], v245, s[48:51], s68 offen sc1\n"
      "s_waitcnt vmcnt(2)\n"
      "s_barrier\n"
      "s_mov_b32 m0, s66\n"
      "s_addk_i32 s67, 1792\n"
      "buffer_load_dwordx4 v244, s[44:47], s67 offen lds\n"
      "s_addk_i32 m0, 4096\n"
      "s_mov_b32 s67, 229632\n"
      "buffer_load_dwordx4 v244, s[44:47], s67 offen lds\n"
      "s_addk_i32 m0, 4096\n"
      "s_addk_i32 s67, 1792\n"
      "buffer_load_dwordx4 v244, s[44:47], s67 offen lds\n"
      "s_addk_i32 m0, 4096\n"
      "s_mov_b32 s67, 286976\n"
      "buffer_load_dwordx4 v244, s[44:47], s67 offen lds\n"
      "s_addk_i32 m0, 4096\n"
      "s_addk_i32 s67, 1792\n"
      "buffer_load_dwordx4 v244, s[44:47], s67 offen lds\n"
      "s_addk_i32 m0, 4096\n"
      "s_mov_b32 s67, 344320\n"
      "buffer_load_dwordx4 v244, s[44:47], s67 offen lds\n"
      "s_addk_i32 m0, 4096\n"
      "s_addk_i32 s67, 1792\n"
      "buffer_load_dwordx4 v244, s[44:47], s67 offen lds\n"
      "ds_read2_b32 v[8:9], v247 offset0:0 offset1:66\n"
      "ds_read2_b32 v[10:11], v247 offset0:16 offset1:82\n"
      "ds_read2_b32 v[12:13], v247 offset0:8 offset1:74\n"
      "ds_read2_b32 v[14:15], v247 offset0:24 offset1:90\n"
      "s_movk_i32 s68, 3584\n"
      "s_waitcnt lgkmcnt(2)\n"
      "buffer_store_dwordx4 v[8:11], v245, s[48:51], s68 offen sc1\n"
      "s_movk_i32 s68, 17920\n"
      "s_waitcnt lgkmcnt(0)\n"
      "buffer_store_dwordx4 v[12:15], v245, s[48:51], s68 offen sc1\n"
      "s_waitcnt vmcnt(2)\n"
      "s_barrier\n"
      "s_addk_i32 m0, 4096\n"
      "s_movk_i32 s67, 384\n"
      "buffer_load_dwordx4 v244, s[44:47], s67 offen lds\n"
      "s_addk_i32 m0, 4096\n"
      "s_movk_i32 s67, 2176\n"
      "buffer_load_dwordx4 v244, s[44:47], s67 offen lds\n"
      "s_addk_i32 m0, 4096\n"
      "s_mov_b32 s67, 57728\n"
      "buffer_load_dwordx4 v244, s[44:47], s67 offen lds\n"
      "s_addk_i32 m0, 4096\n"
      "s_addk_i32 s67, 1792\n"
      "buffer_load_dwordx4 v244, s[44:47], s67 offen lds\n"
      "s_addk_i32 m0, 4096\n"
      "s_mov_b32 s67, 115072\n"
      "buffer_load_dwordx4 v244, s[44:47], s67 offen lds\n"
      "s_addk_i32 m0, 4096\n"
      "s_addk_i32 s67, 1792\n"
      "buffer_load_dwordx4 v244, s[44:47], s67 offen lds\n"
      "s_addk_i32 m0, 4096\n"
      "s_mov_b32 s67, 172416\n"
      "buffer_load_dwordx4 v244, s[44:47], s67 offen lds\n"
      "ds_read2_b32 v[0:1], v248 offset0:0 offset1:66\n"
      "ds_read2_b32 v[2:3], v248 offset0:16 offset1:82\n"
      "ds_read2_b32 v[4:5], v248 offset0:8 offset1:74\n"
      "ds_read2_b32 v[6:7], v248 offset0:24 offset1:90\n"
      "s_movk_i32 s68, 128\n"
      "s_waitcnt lgkmcnt(2)\n"
      "buffer_store_dwordx4 v[0:3], v245, s[48:51], s68 offen sc1\n"
      "s_movk_i32 s68, 14464\n"
      "s_waitcnt lgkmcnt(0)\n"
      "buffer_store_dwordx4 v[4:7], v245, s[48:51], s68 offen sc1\n"
      "s_waitcnt vmcnt(2)\n"
      "s_barrier\n"
      "s_add_u32 m0, s66, 92160\n"
      "s_addk_i32 s67, 1792\n"
      "buffer_load_dwordx4 v244, s[44:47], s67 offen lds\n"
      "s_addk_i32 m0, 4096\n"
      "s_mov_b32 s67, 229760\n"
      "buffer_load_dwordx4 v244, s[44:47], s67 offen lds\n"
      "s_addk_i32 m0, 4096\n"
      "s_addk_i32 s67, 1792\n"
      "buffer_load_dwordx4 v244, s[44:47], s67 offen lds\n"
      "s_addk_i32 m0, 4096\n"
      "s_mov_b32 s67, 287104\n"
      "buffer_load_dwordx4 v244, s[44:47], s67 offen lds\n"
      "s_addk_i32 m0, 4096\n"
      "s_addk_i32 s67, 1792\n"
      "buffer_load_dwordx4 v244, s[44:47], s67 offen lds\n"
      "s_addk_i32 m0, 4096\n"
      "s_mov_b32 s67, 344448\n"
      "buffer_load_dwordx4 v244, s[44:47], s67 offen lds\n"
      "s_addk_i32 m0, 4096\n"
      "s_addk_i32 s67, 1792\n"
      "buffer_load_dwordx4 v244, s[44:47], s67 offen lds\n"
      "ds_read2_b32 v[8:9], v239 offset0:0 offset1:66\n"
      "ds_read2_b32 v[10:11], v239 offset0:16 offset1:82\n"
      "ds_read2_b32 v[12:13], v239 offset0:8 offset1:74\n"
      "ds_read2_b32 v[14:15], v239 offset0:24 offset1:90\n"
      "s_movk_i32 s68, 3712\n"
      "s_waitcnt lgkmcnt(2)\n"
      "buffer_store_dwordx4 v[8:11], v245, s[48:51], s68 offen sc1\n"
      "s_movk_i32 s68, 18048\n"
      "s_waitcnt lgkmcnt(0)\n"
      "buffer_store_dwordx4 v[12:15], v245, s[48:51], s68 offen sc1\n"
      "s_waitcnt vmcnt(2)\n"
      "s_barrier\n"
      "s_addk_i32 m0, 4096\n"
      "s_movk_i32 s67, 512\n"
      "buffer_load_dwordx4 v244, s[44:47], s67 offen lds\n"
      "s_addk_i32 m0, 4096\n"
      "s_movk_i32 s67, 2304\n"
      "buffer_load_dwordx4 v244, s[44:47], s67 offen lds\n"
      "s_addk_i32 m0, 4096\n"
      "s_mov_b32 s67, 57856\n"
      "buffer_load_dwordx4 v244, s[44:47], s67 offen lds\n"
      "s_addk_i32 m0, 4096\n"
      "s_addk_i32 s67, 1792\n"
      "buffer_load_dwordx4 v244, s[44:47], s67 offen lds\n"
      "s_addk_i32 m0, 4096\n"
      "s_mov_b32 s67, 115200\n"
      "buffer_load_dwordx4 v244, s[44:47], s67 offen lds\n"
      "s_addk_i32 m0, 4096\n"
      "s_addk_i32 s67, 1792\n"
      "buffer_load_dwordx4 v244, s[44:47], s67 offen lds\n"
      "s_addk_i32 m0, 4096\n"
      "s_mov_b32 s67, 172544\n"
      "buffer_load_dwordx4 v244, s[44:47], s67 offen lds\n"
      "ds_read2_b32 v[0:1], v246 offset0:0 offset1:66\n"
      "ds_read2_b32 v[2:3], v246 offset0:16 offset1:82\n"
      "ds_read2_b32 v[4:5], v246 offset0:8 offset1:74\n"
      "ds_read2_b32 v[6:7], v246 offset0:24 offset1:90\n"
      "s_movk_i32 s68, 256\n"
      "s_waitcnt lgkmcnt(2)\n"
      "buffer_store_dwordx4 v[0:3], v245, s[48:51], s68 offen sc1\n"
      "s_movk_i32 s68, 14592\n"
      "s_waitcnt lgkmcnt(0)\n"
      "buffer_store_dwordx4 v[4:7], v245, s[48:51], s68 offen sc1\n"
      "s_waitcnt vmcnt(2)\n"
      "s_barrier\n"
      "s_mov_b32 m0, s66\n"
      "s_addk_i32 s67, 1792\n"
      "buffer_load_dwordx4 v244, s[44:47], s67 offen lds\n"
      "s_addk_i32 m0, 4096\n"
      "s_mov_b32 s67, 229888\n"
      "buffer_load_dwordx4 v244, s[44:47], s67 offen lds\n"
      "s_addk_i32 m0, 4096\n"
      "s_addk_i32 s67, 1792\n"
      "buffer_load_dwordx4 v244, s[44:47], s67 offen lds\n"
      "s_addk_i32 m0, 4096\n"
      "s_mov_b32 s67, 287232\n"
      "buffer_load_dwordx4 v244, s[44:47], s67 offen lds\n"
      "s_addk_i32 m0, 4096\n"
      "s_addk_i32 s67, 1792\n"
      "buffer_load_dwordx4 v244, s[44:47], s67 offen lds\n"
      "s_addk_i32 m0, 4096\n"
      "s_mov_b32 s67, 344576\n"
      "buffer_load_dwordx4 v244, s[44:47], s67 offen lds\n"
      "s_addk_i32 m0, 4096\n"
      "s_addk_i32 s67, 1792\n"
      "buffer_load_dwordx4 v244, s[44:47], s67 offen lds\n"
      "ds_read2_b32 v[8:9], v247 offset0:0 offset1:66\n"
      "ds_read2_b32 v[10:11], v247 offset0:16 offset1:82\n"
      "ds_read2_b32 v[12:13], v247 offset0:8 offset1:74\n"
      "ds_read2_b32 v[14:15], v247 offset0:24 offset1:90\n"
      "s_movk_i32 s68, 3840\n"
      "s_waitcnt lgkmcnt(2)\n"
      "buffer_store_dwordx4 v[8:11], v245, s[48:51], s68 offen sc1\n"
      "s_movk_i32 s68, 18176\n"
      "s_waitcnt lgkmcnt(0)\n"
      "buffer_store_dwordx4 v[12:15], v245, s[48:51], s68 offen sc1\n"
      "s_waitcnt vmcnt(2)\n"
      "s_barrier\n"
      "s_addk_i32 m0, 4096\n"
      "s_movk_i32 s67, 640\n"
      "buffer_load_dwordx4 v244, s[44:47], s67 offen lds\n"
      "s_addk_i32 m0, 4096\n"
      "s_movk_i32 s67, 2432\n"
      "buffer_load_dwordx4 v244, s[44:47], s67 offen lds\n"
      "s_addk_i32 m0, 4096\n"
      "s_mov_b32 s67, 57984\n"
      "buffer_load_dwordx4 v244, s[44:47], s67 offen lds\n"
      "s_addk_i32 m0, 4096\n"
      "s_addk_i32 s67, 1792\n"
      "buffer_load_dwordx4 v244, s[44:47], s67 offen lds\n"
      "s_addk_i32 m0, 4096\n"
      "s_mov_b32 s67, 115328\n"
      "buffer_load_dwordx4 v244, s[44:47], s67 offen lds\n"
      "s_addk_i32 m0, 4096\n"
      "s_addk_i32 s67, 1792\n"
      "buffer_load_dwordx4 v244, s[44:47], s67 offen lds\n"
      "s_addk_i32 m0, 4096\n"
      "s_mov_b32 s67, 172672\n"
      "buffer_load_dwordx4 v244, s[44:47], s67 offen lds\n"
      "ds_read2_b32 v[0:1], v248 offset0:0 offset1:66\n"
      "ds_read2_b32 v[2:3], v248 offset0:16 offset1:82\n"
      "ds_read2_b32 v[4:5], v248 offset0:8 offset1:74\n"
      "ds_read2_b32 v[6:7], v248 offset0:24 offset1:90\n"
      "s_movk_i32 s68, 384\n"
      "s_waitcnt lgkmcnt(2)\n"
      "buffer_store_dwordx4 v[0:3], v245, s[48:51], s68 offen sc1\n"
      "s_movk_i32 s68, 14720\n"
      "s_waitcnt lgkmcnt(0)\n"
      "buffer_store_dwordx4 v[4:7], v245, s[48:51], s68 offen sc1\n"
      "s_waitcnt vmcnt(2)\n"
      "s_barrier\n"
      "s_add_u32 m0, s66, 92160\n"
      "s_addk_i32 s67, 1792\n"
      "buffer_load_dwordx4 v244, s[44:47], s67 offen lds\n"
      "s_addk_i32 m0, 4096\n"
      "s_mov_b32 s67, 230016\n"
      "buffer_load_dwordx4 v244, s[44:47], s67 offen lds\n"
      "s_addk_i32 m0, 4096\n"
      "s_addk_i32 s67, 1792\n"
      "buffer_load_dwordx4 v244, s[44:47], s67 offen lds\n"
      "s_addk_i32 m0, 4096\n"
      "s_mov_b32 s67, 287360\n"
      "buffer_load_dwordx4 v244, s[44:47], s67 offen lds\n"
      "s_addk_i32 m0, 4096\n"
      "s_addk_i32 s67, 1792\n"
      "buffer_load_dwordx4 v244, s[44:47], s67 offen lds\n"
      "s_addk_i32 m0, 4096\n"
      "s_mov_b32 s67, 344704\n"
      "buffer_load_dwordx4 v244, s[44:47], s67 offen lds\n"
      "s_addk_i32 m0, 4096\n"
      "s_addk_i32 s67, 1792\n"
      "buffer_load_dwordx4 v244, s[44:47], s67 offen lds\n"
      "ds_read2_b32 v[8:9], v239 offset0:0 offset1:66\n"
      "ds_read2_b32 v[10:11], v239 offset0:16 offset1:82\n"
      "ds_read2_b32 v[12:13], v239 offset0:8 offset1:74\n"
      "ds_read2_b32 v[14:15], v239 offset0:24 offset1:90\n"
      "s_movk_i32 s68, 3968\n"
      "s_waitcnt lgkmcnt(2)\n"
      "buffer_store_dwordx4 v[8:11], v245, s[48:51], s68 offen sc1\n"
      "s_movk_i32 s68, 18304\n"
      "s_waitcnt lgkmcnt(0)\n"
      "buffer_store_dwordx4 v[12:15], v245, s[48:51], s68 offen sc1\n"
      "s_waitcnt vmcnt(2)\n"
      "s_barrier\n"
      "s_addk_i32 m0, 4096\n"
      "s_movk_i32 s67, 768\n"
      "buffer_load_dwordx4 v244, s[44:47], s67 offen lds\n"
      "s_addk_i32 m0, 4096\n"
      "s_movk_i32 s67, 2560\n"
      "buffer_load_dwordx4 v244, s[44:47], s67 offen lds\n"
      "s_addk_i32 m0, 4096\n"
      "s_mov_b32 s67, 58112\n"
      "buffer_load_dwordx4 v244, s[44:47], s67 offen lds\n"
      "s_addk_i32 m0, 4096\n"
      "s_addk_i32 s67, 1792\n"
      "buffer_load_dwordx4 v244, s[44:47], s67 offen lds\n"
      "s_addk_i32 m0, 4096\n"
      "s_mov_b32 s67, 115456\n"
      "buffer_load_dwordx4 v244, s[44:47], s67 offen lds\n"
      "s_addk_i32 m0, 4096\n"
      "s_addk_i32 s67, 1792\n"
      "buffer_load_dwordx4 v244, s[44:47], s67 offen lds\n"
      "s_addk_i32 m0, 4096\n"
      "s_mov_b32 s67, 172800\n"
      "buffer_load_dwordx4 v244, s[44:47], s67 offen lds\n"
      "ds_read2_b32 v[0:1], v246 offset0:0 offset1:66\n"
      "ds_read2_b32 v[2:3], v246 offset0:16 offset1:82\n"
      "ds_read2_b32 v[4:5], v246 offset0:8 offset1:74\n"
      "ds_read2_b32 v[6:7], v246 offset0:24 offset1:90\n"
      "s_movk_i32 s68, 512\n"
      "s_waitcnt lgkmcnt(2)\n"
      "buffer_store_dwordx4 v[0:3], v245, s[48:51], s68 offen sc1\n"
      "s_movk_i32 s68, 14848\n"
      "s_waitcnt lgkmcnt(0)\n"
      "buffer_store_dwordx4 v[4:7], v245, s[48:51], s68 offen sc1\n"
      "s_waitcnt vmcnt(2)\n"
      "s_barrier\n"
      "s_mov_b32 m0, s66\n"
      "s_addk_i32 s67, 1792\n"
      "buffer_load_dwordx4 v244, s[44:47], s67 offen lds\n"
      "s_addk_i32 m0, 4096\n"
      "s_mov_b32 s67, 230144\n"
      "buffer_load_dwordx4 v244, s[44:47], s67 offen lds\n"
      "s_addk_i32 m0, 4096\n"
      "s_addk_i32 s67, 1792\n"
      "buffer_load_dwordx4 v244, s[44:47], s67 offen lds\n"
      "s_addk_i32 m0, 4096\n"
      "s_mov_b32 s67, 287488\n"
      "buffer_load_dwordx4 v244, s[44:47], s67 offen lds\n"
      "s_addk_i32 m0, 4096\n"
      "s_addk_i32 s67, 1792\n"
      "buffer_load_dwordx4 v244, s[44:47], s67 offen lds\n"
      "s_addk_i32 m0, 4096\n"
      "s_mov_b32 s67, 344832\n"
      "buffer_load_dwordx4 v244, s[44:47], s67 offen lds\n"
      "s_addk_i32 m0, 4096\n"
      "s_addk_i32 s67, 1792\n"
      "buffer_load_dwordx4 v244, s[44:47], s67 offen lds\n"
      "ds_read2_b32 v[8:9], v247 offset0:0 offset1:66\n"
      "ds_read2_b32 v[10:11], v247 offset0:16 offset1:82\n"
      "ds_read2_b32 v[12:13], v247 offset0:8 offset1:74\n"
      "ds_read2_b32 v[14:15], v247 offset0:24 offset1:90\n"
      "s_movk_i32 s68, 4096\n"
      "s_waitcnt lgkmcnt(2)\n"
      "buffer_store_dwordx4 v[8:11], v245, s[48:51], s68 offen sc1\n"
      "s_movk_i32 s68, 18432\n"
      "s_waitcnt lgkmcnt(0)\n"
      "buffer_store_dwordx4 v[12:15], v245, s[48:51], s68 offen sc1\n"
      "s_waitcnt vmcnt(2)\n"
      "s_barrier\n"
      "ds_read2_b32 v[0:1], v248 offset0:0 offset1:66\n"
      "ds_read2_b32 v[2:3], v248 offset0:16 offset1:82\n"
      "ds_read2_b32 v[4:5], v248 offset0:8 offset1:74\n"
      "ds_read2_b32 v[6:7], v248 offset0:24 offset1:90\n"
      "s_movk_i32 s68, 640\n"
      "s_waitcnt lgkmcnt(2)\n"
      "buffer_store_dwordx4 v[0:3], v245, s[48:51], s68 offen sc1\n"
      "s_movk_i32 s68, 14976\n"
      "s_waitcnt lgkmcnt(0)\n"
      "buffer_store_dwordx4 v[4:7], v245, s[48:51], s68 offen sc1\n"
      "s_barrier\n"
      "ds_read2_b32 v[8:9], v239 offset0:0 offset1:66\n"
      "ds_read2_b32 v[10:11], v239 offset0:16 offset1:82\n"
      "ds_read2_b32 v[12:13], v239 offset0:8 offset1:74\n"
      "ds_read2_b32 v[14:15], v239 offset0:24 offset1:90\n"
      "s_movk_i32 s68, 4224\n"
      "s_waitcnt lgkmcnt(2)\n"
      "buffer_store_dwordx4 v[8:11], v245, s[48:51], s68 offen sc1\n"
      "s_movk_i32 s68, 18560\n"
      "s_waitcnt lgkmcnt(0)\n"
      "buffer_store_dwordx4 v[12:15], v245, s[48:51], s68 offen sc1\n"
      "s_barrier\n"
      "ds_read2_b32 v[0:1], v246 offset0:0 offset1:66\n"
      "ds_read2_b32 v[2:3], v246 offset0:16 offset1:82\n"
      "ds_read2_b32 v[4:5], v246 offset0:8 offset1:74\n"
      "ds_read2_b32 v[6:7], v246 offset0:24 offset1:90\n"
      "s_movk_i32 s68, 768\n"
      "s_waitcnt lgkmcnt(2)\n"
      "buffer_store_dwordx4 v[0:3], v245, s[48:51], s68 offen sc1\n"
      "s_movk_i32 s68, 15104\n"
      "s_waitcnt lgkmcnt(0)\n"
      "buffer_store_dwordx4 v[4:7], v245, s[48:51], s68 offen sc1\n"
      "s_endpgm\n"
      ".p2align 6\n"
      ".Lm2:\n"
      "s_barrier\n"
      "s_setprio 1\n"
      "s_lshr_b32 s71, %[w], 1\n"
      "s_cmp_lg_u32 s71, 1\n"
      "s_cbranch_scc1 .Lfr7\n"
      "ds_read_b128 v[168:171], v228 offset:32256\n"
      "ds_read_b128 v[172:175], v228 offset:32512\n"
      "ds_read_b128 v[176:179], v228 offset:32768\n"
      "ds_read_b128 v[180:183], v228 offset:33024\n"
      "ds_read_b128 v[184:187], v228 offset:33280\n"
      "ds_read_b128 v[188:191], v228 offset:33536\n"
      "ds_read_b128 v[192:195], v228 offset:33792\n"
      "ds_read_b128 v[196:199], v228 offset:34048\n"
      "ds_read_b128 v[200:203], v228 offset:34304\n"
      "ds_read_b128 v[204:207], v228 offset:34560\n"
      "ds_read_b128 v[208:211], v228 offset:34816\n"
      "ds_read_b128 v[212:215], v228 offset:35072\n"
      "ds_read_b128 v[216:219], v228 offset:35328\n"
      "ds_read_b128 v[220:223], v228 offset:35584\n"
      ".Lfr7:\n"
      "ds_read_b64_tr_b16 v[240:241], v254 offset:28672\n"
      "ds_read_b64_tr_b16 v[242:243], v254 offset:30720\n"
      "ds_read_b64_tr_b16 v[244:245], v254 offset:32768\n"
      "ds_read_b64_tr_b16 v[246:247], v254 offset:34816\n"
      "s_waitcnt lgkmcnt(2)\n"
      "v_mfma_f32_16x16x32_bf16 v[224:227], v[240:243], v[0:3], 0\n"
      "ds_read_b64_tr_b16 v[248:249], v254 offset:36864\n"
      "v_mfma_f32_16x16x32_bf16 v[228:231], v[240:243], v[56:59], 0\n"
      "v_mfma_f32_16x16x32_bf16 v[232:235], v[240:243], v[112:115], 0\n"
      "ds_read_b64_tr_b16 v[250:251], v254 offset:38912\n"
      "v_mfma_f32_16x16x32_bf16 v[236:239], v[240:243], v[168:171], 0\n"
      "s_waitcnt lgkmcnt(2)\n"
      "v_mfma_f32_16x16x32_bf16 v[224:227], v[244:247], v[4:7], v[224:227]\n"
      "ds_read_b64_tr_b16 v[240:241], v254 offset:40960\n"
      "v_mfma_f32_16x16x32_bf16 v[228:231], v[244:247], v[60:63], v[228:231]\n"
      "v_mfma_f32_16x16x32_bf16 v[232:235], v[244:247], v[116:119], v[232:235]\n"
      "ds_read_b64_tr_b16 v[242:243], v254 offset:43008\n"
      "v_mfma_f32_16x16x32_bf16 v[236:239], v[244:247], v[172:175], v[236:239]\n"
      "s_waitcnt lgkmcnt(2)\n"
      "v_mfma_f32_16x16x32_bf16 v[224:227], v[248:251], v[8:11], v[224:227]\n"
      "ds_read_b64_tr_b16 v[244:245], v254 offset:45056\n"
      "v_mfma_f32_16x16x32_bf16 v[228:231], v[248:251], v[64:67], v[228:231]\n"
      "v_mfma_f32_16x16x32_bf16 v[232:235], v[248:251], v[120:123], v[232:235]\n"
      "ds_read_b64_tr_b16 v[246:247], v254 offset:47104\n"
      "v_mfma_f32_16x16x32_bf16 v[236:239], v[248:251], v[176:179], v[236:239]\n"
      "s_waitcnt lgkmcnt(2)\n"
      "v_mfma_f32_16x16x32_bf16 v[224:227], v[240:243], v[12:15], v[224:227]\n"
      "ds_read_b64_tr_b16 v[248:249], v254 offset:49152\n"
      "v_mfma_f32_16x16x32_bf16 v[228:231], v[240:243], v[68:71], v[228:231]\n"
      "s_waitcnt vmcnt(1)\n"
      "s_barrier\n"
      "v_mfma_f32_16x16x32_bf16 v[232:235], v[240:243], v[124:127], v[232:235]\n"
      "ds_read_b64_tr_b16 v[250:251], v254 offset:51200\n"
      "v_mfma_f32_16x16x32_bf16 v[236:239], v[240:243], v[180:183], v[236:239]\n"
      "s_waitcnt lgkmcnt(2)\n"
      "v_mfma_f32_16x16x32_bf16 v[224:227], v[244:247], v[16:19], v[224:227]\n"
      "ds_read_b64_tr_b16 v[240:241], v254 offset:53248\n"
      "v_mfma_f32_16x16x32_bf16 v[228:231], v[244:247], v[72:75], v[228:231]\n"
      "v_mfma_f32_16x16x32_bf16 v[232:235], v[244:247], v[128:131], v[232:235]\n"
      "ds_read_b64_tr_b16 v[242:243], v254 offset:55296\n"
      "v_mfma_f32_16x16x32_bf16 v[236:239], v[244:247], v[184:187], v[236:239]\n"
      "s_waitcnt lgkmcnt(2)\n"
      "v_mfma_f32_16x16x32_bf16 v[224:227], v[248:251], v[20:23], v[224:227]\n"
      "ds_read_b64_tr_b16 v[244:245], v252 offset:0\n"
      "v_mfma_f32_16x16x32_bf16 v[228:231], v[248:251], v[76:79], v[228:231]\n"
      "v_mfma_f32_16x16x32_bf16 v[232:235], v[248:251], v[132:135], v[232:235]\n"
      "ds_read_b64_tr_b16 v[246:247], v252 offset:2048\n"
      "v_mfma_f32_16x16x32_bf16 v[236:239], v[248:251], v[188:191], v[236:239]\n"
      "s_waitcnt lgkmcnt(2)\n"
      "v_mfma_f32_16x16x32_bf16 v[224:227], v[240:243], v[24:27], v[224:227]\n"
      "ds_read_b64_tr_b16 v[248:249], v252 offset:4096\n"
      "v_mfma_f32_16x16x32_bf16 v[228:231], v[240:243], v[80:83], v[228:231]\n"
      "v_mfma_f32_16x16x32_bf16 v[232:235], v[240:243], v[136:139], v[232:235]\n"
      "ds_read_b64_tr_b16 v[250:251], v252 offset:6144\n"
      "v_mfma_f32_16x16x32_bf16 v[236:239], v[240:243], v[192:195], v[236:239]\n"
      "s_waitcnt lgkmcnt(2)\n"
      "v_mfma_f32_16x16x32_bf16 v[224:227], v[244:247], v[28:31], v[224:227]\n"
      "ds_read_b64_tr_b16 v[240:241], v252 offset:8192\n"
      "v_mfma_f32_16x16x32_bf16 v[228:231], v[244:247], v[84:87], v[228:231]\n"
      "v_mfma_f32_16x16x32_bf16 v[232:235], v[244:247], v[140:143], v[232:235]\n"
      "ds_read_b64_tr_b16 v[242:243], v252 offset:10240\n"
      "v_mfma_f32_16x16x32_bf16 v[236:239], v[244:247], v[196:199], v[236:239]\n"
      "s_waitcnt lgkmcnt(2)\n"
      "v_mfma_f32_16x16x32_bf16 v[224:227], v[248:251], v[32:35], v[224:227]\n"
      "ds_read_b64_tr_b16 v[244:245], v252 offset:12288\n"
      "v_mfma_f32_16x16x32_bf16 v[228:231], v[248:251], v[88:91], v[228:231]\n"
      "v_mfma_f32_16x16x32_bf16 v[232:235], v[248:251], v[144:147], v[232:235]\n"
      "ds_read_b64_tr_b16 v[246:247], v252 offset:14336\n"
      "v_mfma_f32_16x16x32_bf16 v[236:239], v[248:251], v[200:203], v[236:239]\n"
      "s_waitcnt lgkmcnt(2)\n"
      "v_mfma_f32_16x16x32_bf16 v[224:227], v[240:243], v[36:39], v[224:227]\n"
      "ds_read_b64_tr_b16 v[248:249], v252 offset:16384\n"
      "v_mfma_f32_16x16x32_bf16 v[228:231], v[240:243], v[92:95], v[228:231]\n"
      "v_mfma_f32_16x16x32_bf16 v[232:235], v[240:243], v[148:151], v[232:235]\n"
      "ds_read_b64_tr_b16 v[250:251], v252 offset:18432\n"
      "v_mfma_f32_16x16x32_bf16 v[236:239], v[240:243], v[204:207], v[236:239]\n"
      "s_waitcnt lgkmcnt(2)\n"
      "v_mfma_f32_16x16x32_bf16 v[224:227], v[244:247], v[40:43], v[224:227]\n"
      "ds_read_b64_tr_b16 v[240:241], v252 offset:20480\n"
      "v_mfma_f32_16x16x32_bf16 v[228:231], v[244:247], v[96:99], v[228:231]\n"
      "v_mfma_f32_16x16x32_bf16 v[232:235], v[244:247], v[152:155], v[232:235]\n"
      "ds_read_b64_tr_b16 v[242:243], v252 offset:22528\n"
      "v_mfma_f32_16x16x32_bf16 v[236:239], v[244:247], v[208:211], v[236:239]\n"
      "s_waitcnt lgkmcnt(2)\n"
      "v_mfma_f32_16x16x32_bf16 v[224:227], v[248:251], v[44:47], v[224:227]\n"
      "ds_read_b64_tr_b16 v[244:245], v252 offset:24576\n"
      "v_mfma_f32_16x16x32_bf16 v[228:231], v[248:251], v[100:103], v[228:231]\n"
      "v_mfma_f32_16x16x32_bf16 v[232:235], v[248:251], v[156:159], v[232:235]\n"
      "ds_read_b64_tr_b16 v[246:247], v252 offset:26624\n"
      "v_mfma_f32_16x16x32_bf16 v[236:239], v[248:251], v[212:215], v[236:239]\n"
      "s_waitcnt lgkmcnt(2)\n"
      "v_mfma_f32_16x16x32_bf16 v[224:227], v[240:243], v[48:51], v[224:227]\n"
      "ds_read_b64_tr_b16 v[248:249], v255 offset:28672\n"
      "v_mfma_f32_16x16x32_bf16 v[228:231], v[240:243], v[104:107], v[228:231]\n"
      "v_mfma_f32_16x16x32_bf16 v[232:235], v[240:243], v[160:163], v[232:235]\n"
      "ds_read_b64_tr_b16 v[250:251], v255 offset:30720\n"
      "v_mfma_f32_16x16x32_bf16 v[236:239], v[240:243], v[216:219], v[236:239]\n"
      "s_waitcnt lgkmcnt(2)\n"
      "v_mfma_f32_16x16x32_bf16 v[224:227], v[244:247], v[52:55], v[224:227]\n"
      "ds_read_b64_tr_b16 v[240:241], v255 offset:32768\n"
      "v_mfma_f32_16x16x32_bf16 v[228:231], v[244:247], v[108:111], v[228:231]\n"
      "v_mfma_f32_16x16x32_bf16 v[232:235], v[244:247], v[164:167], v[232:235]\n"
      "ds_read_b64_tr_b16 v[242:243], v255 offset:34816\n"
      "v_mfma_f32_16x16x32_bf16 v[236:239], v[244:247], v[220:223], v[236:239]\n"
      "s_nop 2\n"
      "v_cvt_pk_bf16_f32 v224, v224, v225\n"
      "v_cvt_pk_bf16_f32 v225, v226, v227\n"
      "v_cvt_pk_bf16_f32 v228, v228, v229\n"
      "v_cvt_pk_bf16_f32 v229, v230, v231\n"
      "s_add_u32 m0, s38, 40000\n"
      "s_nop 0\n"
      "ds_write_addtid_b32 v224 offset:17344\n"
      "ds_write_addtid_b32 v225 offset:17608\n"
      "s_waitcnt lgkmcnt(4)\n"
      "v_mfma_f32_16x16x32_bf16 v[224:227], v[248:251], v[0:3], 0\n"
      "ds_write_addtid_b32 v228 offset:19520\n"
      "ds_write_addtid_b32 v229 offset:19784\n"
      "v_cvt_pk_bf16_f32 v232, v232, v233\n"
      "v_cvt_pk_bf16_f32 v233, v234, v235\n"
      "v_mfma_f32_16x16x32_bf16 v[228:231], v[248:251], v[56:59], 0\n"
      "ds_write_addtid_b32 v232 offset:21696\n"
      "ds_write_addtid_b32 v233 offset:21960\n"
      "v_cvt_pk_bf16_f32 v236, v236, v237\n"
      "v_cvt_pk_bf16_f32 v237, v238, v239\n"
      "v_mfma_f32_16x16x32_bf16 v[232:235], v[248:251], v[112:115], 0\n"
      "ds_write_addtid_b32 v236 offset:23872\n"
      "ds_write_addtid_b32 v237 offset:24136\n"
      "v_mfma_f32_16x16x32_bf16 v[236:239], v[248:251], v[168:171], 0\n"
      "ds_read_b64_tr_b16 v[244:245], v255 offset:36864\n"
      "ds_read_b64_tr_b16 v[246:247], v255 offset:38912\n"
      "s_waitcnt lgkmcnt(10)\n"
      "v_mfma_f32_16x16x32_bf16 v[224:227], v[240:243], v[4:7], v[224:227]\n"
      "ds_read_b64_tr_b16 v[248:249], v255 offset:40960\n"
      "v_mfma_f32_16x16x32_bf16 v[228:231], v[240:243], v[60:63], v[228:231]\n"
      "s_waitcnt lgkmcnt(3)\n"
      "s_barrier\n"
      "v_mfma_f32_16x16x32_bf16 v[232:235], v[240:243], v[116:119], v[232:235]\n"
      "ds_read_b64_tr_b16 v[250:251], v255 offset:43008\n"
      "v_mfma_f32_16x16x32_bf16 v[236:239], v[240:243], v[172:175], v[236:239]\n"
      "s_waitcnt lgkmcnt(2)\n"
      "v_mfma_f32_16x16x32_bf16 v[224:227], v[244:247], v[8:11], v[224:227]\n"
      "ds_read_b64_tr_b16 v[240:241], v255 offset:45056\n"
      "v_mfma_f32_16x16x32_bf16 v[228:231], v[244:247], v[64:67], v[228:231]\n"
      "v_mfma_f32_16x16x32_bf16 v[232:235], v[244:247], v[120:123], v[232:235]\n"
      "ds_read_b64_tr_b16 v[242:243], v255 offset:47104\n"
      "v_mfma_f32_16x16x32_bf16 v[236:239], v[244:247], v[176:179], v[236:239]\n"
      "s_waitcnt lgkmcnt(2)\n"
      "v_mfma_f32_16x16x32_bf16 v[224:227], v[248:251], v[12:15], v[224:227]\n"
      "ds_read_b64_tr_b16 v[244:245], v255 offset:49152\n"
      "v_mfma_f32_16x16x32_bf16 v[228:231], v[248:251], v[68:71], v[228:231]\n"
      "v_mfma_f32_16x16x32_bf16 v[232:235], v[248:251], v[124:127], v[232:235]\n"
      "ds_read_b64_tr_b16 v[246:247], v255 offset:51200\n"
      "v_mfma_f32_16x16x32_bf16 v[236:239], v[248:251], v[180:183], v[236:239]\n"
      "s_waitcnt lgkmcnt(2)\n"
      "v_mfma_f32_16x16x32_bf16 v[224:227], v[240:243], v[16:19], v[224:227]\n"
      "ds_read_b64_tr_b16 v[248:249], v255 offset:53248\n"
      "v_mfma_f32_16x16x32_bf16 v[228:231], v[240:243], v[72:75], v[228:231]\n"
      "v_mfma_f32_16x16x32_bf16 v[232:235], v[240:243], v[128:131], v[232:235]\n"
      "ds_read_b64_tr_b16 v[250:251], v255 offset:55296\n"
      "v_mfma_f32_16x16x32_bf16 v[236:239], v[240:243], v[184:187], v[236:239]\n"
      "s_waitcnt lgkmcnt(2)\n"
      "v_mfma_f32_16x16x32_bf16 v[224:227], v[244:247], v[20:23], v[224:227]\n"
      "ds_read_b64_tr_b16 v[240:241], v253 offset:0\n"
      "v_mfma_f32_16x16x32_bf16 v[228:231], v[244:247], v[76:79], v[228:231]\n"
      "v_mfma_f32_16x16x32_bf16 v[232:235], v[244:247], v[132:135], v[232:235]\n"
      "ds_read_b64_tr_b16 v[242:243], v253 offset:2048\n"
      "v_mfma_f32_16x16x32_bf16 v[236:239], v[244:247], v[188:191], v[236:239]\n"
      "s_waitcnt lgkmcnt(2)\n"
      "v_mfma_f32_16x16x32_bf16 v[224:227], v[248:251], v[24:27], v[224:227]\n"
      "ds_read_b64_tr_b16 v[244:245], v253 offset:4096\n"
      "v_mfma_f32_16x16x32_bf16 v[228:231], v[248:251], v[80:83], v[228:231]\n"
      "v_mfma_f32_16x16x32_bf16 v[232:235], v[248:251], v[136:139], v[232:235]\n"
      "ds_read_b64_tr_b16 v[246:247], v253 offset:6144\n"
      "v_mfma_f32_16x16x32_bf16 v[236:239], v[248:251], v[192:195], v[236:239]\n"
      "s_waitcnt lgkmcnt(2)\n"
      "v_mfma_f32_16x16x32_bf16 v[224:227], v[240:243], v[28:31], v[224:227]\n"
      "ds_read_b64_tr_b16 v[248:249], v253 offset:8192\n"
      "v_mfma_f32_16x16x32_bf16 v[228:231], v[240:243], v[84:87], v[228:231]\n"
      "v_mfma_f32_16x16x32_bf16 v[232:235], v[240:243], v[140:143], v[232:235]\n"
      "ds_read_b64_tr_b16 v[250:251], v253 offset:10240\n"
      "v_mfma_f32_16x16x32_bf16 v[236:239], v[240:243], v[196:199], v[236:239]\n"
      "s_waitcnt lgkmcnt(2)\n"
      "v_mfma_f32_16x16x32_bf16 v[224:227], v[244:247], v[32:35], v[224:227]\n"
      "ds_read_b64_tr_b16 v[240:241], v253 offset:12288\n"
      "v_mfma_f32_16x16x32_bf16 v[228:231], v[244:247], v[88:91], v[228:231]\n"
      "v_mfma_f32_16x16x32_bf16 v[232:235], v[244:247], v[144:147], v[232:235]\n"
      "ds_read_b64_tr_b16 v[242:243], v253 offset:14336\n"
      "v_mfma_f32_16x16x32_bf16 v[236:239], v[244:247], v[200:203], v[236:239]\n"
      "s_waitcnt lgkmcnt(2)\n"
      "v_mfma_f32_16x16x32_bf16 v[224:227], v[248:251], v[36:39], v[224:227]\n"
      "ds_read_b64_tr_b16 v[244:245], v253 offset:16384\n"
      "v_mfma_f32_16x16x32_bf16 v[228:231], v[248:251], v[92:95], v[228:231]\n"
      "v_mfma_f32_16x16x32_bf16 v[232:235], v[248:251], v[148:151], v[232:235]\n"
      "ds_read_b64_tr_b16 v[246:247], v253 offset:18432\n"
      "v_mfma_f32_16x16x32_bf16 v[236:239], v[248:251], v[204:207], v[236:239]\n"
      "s_waitcnt lgkmcnt(2)\n"
      "v_mfma_f32_16x16x32_bf16 v[224:227], v[240:243], v[40:43], v[224:227]\n"
      "ds_read_b64_tr_b16 v[248:249], v253 offset:20480\n"
      "v_mfma_f32_16x16x32_bf16 v[228:231], v[240:243], v[96:99], v[228:231]\n"
      "v_mfma_f32_16x16x32_bf16 v[232:235], v[240:243], v[152:155], v[232:235]\n"
      "ds_read_b64_tr_b16 v[250:251], v253 offset:22528\n"
      "v_mfma_f32_16x16x32_bf16 v[236:239], v[240:243], v[208:211], v[236:239]\n"
      "s_waitcnt lgkmcnt(2)\n"
      "v_mfma_f32_16x16x32_bf16 v[224:227], v[244:247], v[44:47], v[224:227]\n"
      "ds_read_b64_tr_b16 v[240:241], v253 offset:24576\n"
      "v_mfma_f32_16x16x32_bf16 v[228:231], v[244:247], v[100:103], v[228:231]\n"
      "v_mfma_f32_16x16x32_bf16 v[232:235], v[244:247], v[156:159], v[232:235]\n"
      "ds_read_b64_tr_b16 v[242:243], v253 offset:26624\n"
      "v_mfma_f32_16x16x32_bf16 v[236:239], v[244:247], v[212:215], v[236:239]\n"
      "s_waitcnt lgkmcnt(2)\n"
      "v_mfma_f32_16x16x32_bf16 v[224:227], v[248:251], v[48:51], v[224:227]\n"
      "ds_read_b64_tr_b16 v[244:245], v252 offset:28672\n"
      "v_mfma_f32_16x16x32_bf16 v[228:231], v[248:251], v[104:107], v[228:231]\n"
      "v_mfma_f32_16x16x32_bf16 v[232:235], v[248:251], v[160:163], v[232:235]\n"
      "ds_read_b64_tr_b16 v[246:247], v252 offset:30720\n"
      "v_mfma_f32_16x16x32_bf16 v[236:239], v[248:251], v[216:219], v[236:239]\n"
      "s_waitcnt lgkmcnt(2)\n"
      "v_mfma_f32_16x16x32_bf16 v[224:227], v[240:243], v[52:55], v[224:227]\n"
      "ds_read_b64_tr_b16 v[248:249], v252 offset:32768\n"
      "v_mfma_f32_16x16x32_bf16 v[228:231], v[240:243], v[108:111], v[228:231]\n"
      "v_mfma_f32_16x16x32_bf16 v[232:235], v[240:243], v[164:167], v[232:235]\n"
      "ds_read_b64_tr_b16 v[250:251], v252 offset:34816\n"
      "v_mfma_f32_16x16x32_bf16 v[236:239], v[240:243], v[220:223], v[236:239]\n"
      "s_nop 2\n"
      "v_cvt_pk_bf16_f32 v224, v224, v225\n"
      "v_cvt_pk_bf16_f32 v225, v226, v227\n"
      "v_cvt_pk_bf16_f32 v228, v228, v229\n"
      "v_cvt_pk_bf16_f32 v229, v230, v231\n"
      "ds_write_addtid_b32 v224 offset:18400\n"
      "ds_write_addtid_b32 v225 offset:18664\n"
      "s_waitcnt lgkmcnt(4)\n"
      "v_mfma_f32_16x16x32_bf16 v[224:227], v[244:247], v[0:3], 0\n"
      "ds_write_addtid_b32 v228 offset:20576\n"
      "ds_write_addtid_b32 v229 offset:20840\n"
      "v_cvt_pk_bf16_f32 v232, v232, v233\n"
      "v_cvt_pk_bf16_f32 v233, v234, v235\n"
      "v_mfma_f32_16x16x32_bf16 v[228:231], v[244:247], v[56:59], 0\n"
      "ds_write_addtid_b32 v232 offset:22752\n"
      "ds_write_addtid_b32 v233 offset:23016\n"
      "v_cvt_pk_bf16_f32 v236, v236, v237\n"
      "v_cvt_pk_bf16_f32 v237, v238, v239\n"
      "v_mfma_f32_16x16x32_bf16 v[232:235], v[244:247], v[112:115], 0\n"
      "ds_write_addtid_b32 v236 offset:24928\n"
      "ds_write_addtid_b32 v237 offset:25192\n"
      "v_mfma_f32_16x16x32_bf16 v[236:239], v[244:247], v[168:171], 0\n"
      "ds_read_b64_tr_b16 v[240:241], v252 offset:36864\n"
      "ds_read_b64_tr_b16 v[242:243], v252 offset:38912\n"
      "s_waitcnt lgkmcnt(10)\n"
      "v_mfma_f32_16x16x32_bf16 v[224:227], v[248:251], v[4:7], v[224:227]\n"
      "ds_read_b64_tr_b16 v[244:245], v252 offset:40960\n"
      "v_mfma_f32_16x16x32_bf16 v[228:231], v[248:251], v[60:63], v[228:231]\n"
      "s_waitcnt lgkmcnt(3)\n"
      "s_barrier\n"
      "v_mfma_f32_16x16x32_bf16 v[232:235], v[248:251], v[116:119], v[232:235]\n"
      "ds_read_b64_tr_b16 v[246:247], v252 offset:43008\n"
      "v_mfma_f32_16x16x32_bf16 v[236:239], v[248:251], v[172:175], v[236:239]\n"
      "s_waitcnt lgkmcnt(2)\n"
      "v_mfma_f32_16x16x32_bf16 v[224:227], v[240:243], v[8:11], v[224:227]\n"
      "ds_read_b64_tr_b16 v[248:249], v252 offset:45056\n"
      "v_mfma_f32_16x16x32_bf16 v[228:231], v[240:243], v[64:67], v[228:231]\n"
      "v_mfma_f32_16x16x32_bf16 v[232:235], v[240:243], v[120:123], v[232:235]\n"
      "ds_read_b64_tr_b16 v[250:251], v252 offset:47104\n"
      "v_mfma_f32_16x16x32_bf16 v[236:239], v[240:243], v[176:179], v[236:239]\n"
      "s_waitcnt lgkmcnt(2)\n"
      "v_mfma_f32_16x16x32_bf16 v[224:227], v[244:247], v[12:15], v[224:227]\n"
      "ds_read_b64_tr_b16 v[240:241], v252 offset:49152\n"
      "v_mfma_f32_16x16x32_bf16 v[228:231], v[244:247], v[68:71], v[228:231]\n"
      "v_mfma_f32_16x16x32_bf16 v[232:235], v[244:247], v[124:127], v[232:235]\n"
      "ds_read_b64_tr_b16 v[242:243], v252 offset:51200\n"
      "v_mfma_f32_16x16x32_bf16 v[236:239], v[244:247], v[180:183], v[236:239]\n"
      "s_waitcnt lgkmcnt(2)\n"
      "v_mfma_f32_16x16x32_bf16 v[224:227], v[248:251], v[16:19], v[224:227]\n"
      "ds_read_b64_tr_b16 v[244:245], v252 offset:53248\n"
      "v_mfma_f32_16x16x32_bf16 v[228:231], v[248:251], v[72:75], v[228:231]\n"
      "v_mfma_f32_16x16x32_bf16 v[232:235], v[248:251], v[128:131], v[232:235]\n"
      "ds_read_b64_tr_b16 v[246:247], v252 offset:55296\n"
      "v_mfma_f32_16x16x32_bf16 v[236:239], v[248:251], v[184:187], v[236:239]\n"
      "s_waitcnt lgkmcnt(2)\n"
      "v_mfma_f32_16x16x32_bf16 v[224:227], v[240:243], v[20:23], v[224:227]\n"
      "ds_read_b64_tr_b16 v[248:249], v254 offset:0\n"
      "v_mfma_f32_16x16x32_bf16 v[228:231], v[240:243], v[76:79], v[228:231]\n"
      "v_mfma_f32_16x16x32_bf16 v[232:235], v[240:243], v[132:135], v[232:235]\n"
      "ds_read_b64_tr_b16 v[250:251], v254 offset:2048\n"
      "v_mfma_f32_16x16x32_bf16 v[236:239], v[240:243], v[188:191], v[236:239]\n"
      "s_waitcnt lgkmcnt(2)\n"
      "v_mfma_f32_16x16x32_bf16 v[224:227], v[244:247], v[24:27], v[224:227]\n"
      "ds_read_b64_tr_b16 v[240:241], v254 offset:4096\n"
      "v_mfma_f32_16x16x32_bf16 v[228:231], v[244:247], v[80:83], v[228:231]\n"
      "v_mfma_f32_16x16x32_bf16 v[232:235], v[244:247], v[136:139], v[232:235]\n"
      "ds_read_b64_tr_b16 v[242:243], v254 offset:6144\n"
      "v_mfma_f32_16x16x32_bf16 v[236:239], v[244:247], v[192:195], v[236:239]\n"
      "s_waitcnt lgkmcnt(2)\n"
      "v_mfma_f32_16x16x32_bf16 v[224:227], v[248:251], v[28:31], v[224:227]\n"
      "ds_read_b64_tr_b16 v[244:245], v254 offset:8192\n"
      "v_mfma_f32_16x16x32_bf16 v[228:231], v[248:251], v[84:87], v[228:231]\n"
      "v_mfma_f32_16x16x32_bf16 v[232:235], v[248:251], v[140:143], v[232:235]\n"
      "ds_read_b64_tr_b16 v[246:247], v254 offset:10240\n"
      "v_mfma_f32_16x16x32_bf16 v[236:239], v[248:251], v[196:199], v[236:239]\n"
      "s_waitcnt lgkmcnt(2)\n"
      "v_mfma_f32_16x16x32_bf16 v[224:227], v[240:243], v[32:35], v[224:227]\n"
      "ds_read_b64_tr_b16 v[248:249], v254 offset:12288\n"
      "v_mfma_f32_16x16x32_bf16 v[228:231], v[240:243], v[88:91], v[228:231]\n"
      "v_mfma_f32_16x16x32_bf16 v[232:235], v[240:243], v[144:147], v[232:235]\n"
      "ds_read_b64_tr_b16 v[250:251], v254 offset:14336\n"
      "v_mfma_f32_16x16x32_bf16 v[236:239], v[240:243], v[200:203], v[236:239]\n"
      "s_waitcnt lgkmcnt(2)\n"
      "v_mfma_f32_16x16x32_bf16 v[224:227], v[244:247], v[36:39], v[224:227]\n"
      "ds_read_b64_tr_b16 v[240:241], v254 offset:16384\n"
      "v_mfma_f32_16x16x32_bf16 v[228:231], v[244:247], v[92:95], v[228:231]\n"
      "v_mfma_f32_16x16x32_bf16 v[232:235], v[244:247], v[148:151], v[232:235]\n"
      "ds_read_b64_tr_b16 v[242:243], v254 offset:18432\n"
      "v_mfma_f32_16x16x32_bf16 v[236:239], v[244:247], v[204:207], v[236:239]\n"
      "s_waitcnt lgkmcnt(2)\n"
      "v_mfma_f32_16x16x32_bf16 v[224:227], v[248:251], v[40:43], v[224:227]\n"
      "ds_read_b64_tr_b16 v[244:245], v254 offset:20480\n"
      "v_mfma_f32_16x16x32_bf16 v[228:231], v[248:251], v[96:99], v[228:231]\n"
      "v_mfma_f32_16x16x32_bf16 v[232:235], v[248:251], v[152:155], v[232:235]\n"
      "ds_read_b64_tr_b16 v[246:247], v254 offset:22528\n"
      "v_mfma_f32_16x16x32_bf16 v[236:239], v[248:251], v[208:211], v[236:239]\n"
      "s_waitcnt lgkmcnt(2)\n"
      "v_mfma_f32_16x16x32_bf16 v[224:227], v[240:243], v[44:47], v[224:227]\n"
      "ds_read_b64_tr_b16 v[248:249], v254 offset:24576\n"
      "v_mfma_f32_16x16x32_bf16 v[228:231], v[240:243], v[100:103], v[228:231]\n"
      "v_mfma_f32_16x16x32_bf16 v[232:235], v[240:243], v[156:159], v[232:235]\n"
      "ds_read_b64_tr_b16 v[250:251], v254 offset:26624\n"
      "v_mfma_f32_16x16x32_bf16 v[236:239], v[240:243], v[212:215], v[236:239]\n"
      "s_waitcnt lgkmcnt(2)\n"
      "v_mfma_f32_16x16x32_bf16 v[224:227], v[244:247], v[48:51], v[224:227]\n"
      "ds_read_b64_tr_b16 v[240:241], v253 offset:28672\n"
      "v_mfma_f32_16x16x32_bf16 v[228:231], v[244:247], v[104:107], v[228:231]\n"
      "v_mfma_f32_16x16x32_bf16 v[232:235], v[244:247], v[160:163], v[232:235]\n"
      "ds_read_b64_tr_b16 v[242:243], v253 offset:30720\n"
      "v_mfma_f32_16x16x32_bf16 v[236:239], v[244:247], v[216:219], v[236:239]\n"
      "s_waitcnt lgkmcnt(2)\n"
      "v_mfma_f32_16x16x32_bf16 v[224:227], v[248:251], v[52:55], v[224:227]\n"
      "ds_read_b64_tr_b16 v[244:245], v253 offset:32768\n"
      "v_mfma_f32_16x16x32_bf16 v[228:231], v[248:251], v[108:111], v[228:231]\n"
      "v_mfma_f32_16x16x32_bf16 v[232:235], v[248:251], v[164:167], v[232:235]\n"
      "ds_read_b64_tr_b16 v[246:247], v253 offset:34816\n"
      "v_mfma_f32_16x16x32_bf16 v[236:239], v[248:251], v[220:223], v[236:239]\n"
      "s_nop 2\n"
      "v_cvt_pk_bf16_f32 v224, v224, v225\n"
      "v_cvt_pk_bf16_f32 v225, v226, v227\n"
      "v_cvt_pk_bf16_f32 v228, v228, v229\n"
      "v_cvt_pk_bf16_f32 v229, v230, v231\n"
      "ds_write_addtid_b32 v224 offset:34752\n"
      "ds_write_addtid_b32 v225 offset:35016\n"
      "s_waitcnt lgkmcnt(4)\n"
      "v_mfma_f32_16x16x32_bf16 v[224:227], v[240:243], v[0:3], 0\n"
      "ds_write_addtid_b32 v228 offset:36928\n"
      "ds_write_addtid_b32 v229 offset:37192\n"
      "v_cvt_pk_bf16_f32 v232, v232, v233\n"
      "v_cvt_pk_bf16_f32 v233, v234, v235\n"
      "v_mfma_f32_16x16x32_bf16 v[228:231], v[240:243], v[56:59], 0\n"
      "ds_write_addtid_b32 v232 offset:39104\n"
      "ds_write_addtid_b32 v233 offset:39368\n"
      "v_cvt_pk_bf16_f32 v236, v236, v237\n"
      "v_cvt_pk_bf16_f32 v237, v238, v239\n"
      "v_mfma_f32_16x16x32_bf16 v[232:235], v[240:243], v[112:115], 0\n"
      "ds_write_addtid_b32 v236 offset:41280\n"
      "ds_write_addtid_b32 v237 offset:41544\n"
      "v_mfma_f32_16x16x32_bf16 v[236:239], v[240:243], v[168:171], 0\n"
      "ds_read_b64_tr_b16 v[248:249], v253 offset:36864\n"
      "ds_read_b64_tr_b16 v[250:251], v253 offset:38912\n"
      "s_waitcnt lgkmcnt(10)\n"
      "v_mfma_f32_16x16x32_bf16 v[224:227], v[244:247], v[4:7], v[224:227]\n"
      "ds_read_b64_tr_b16 v[240:241], v253 offset:40960\n"
      "v_mfma_f32_16x16x32_bf16 v[228:231], v[244:247], v[60:63], v[228:231]\n"
      "s_waitcnt lgkmcnt(3)\n"
      "s_barrier\n"
      "v_mfma_f32_16x16x32_bf16 v[232:235], v[244:247], v[116:119], v[232:235]\n"
      "ds_read_b64_tr_b16 v[242:243], v253 offset:43008\n"
      "v_mfma_f32_16x16x32_bf16 v[236:239], v[244:247], v[172:175], v[236:239]\n"
      "s_waitcnt lgkmcnt(2)\n"
      "v_mfma_f32_16x16x32_bf16 v[224:227], v[248:251], v[8:11], v[224:227]\n"
      "ds_read_b64_tr_b16 v[244:245], v253 offset:45056\n"
      "v_mfma_f32_16x16x32_bf16 v[228:231], v[248:251], v[64:67], v[228:231]\n"
      "v_mfma_f32_16x16x32_bf16 v[232:235], v[248:251], v[120:123], v[232:235]\n"
      "ds_read_b64_tr_b16 v[246:247], v253 offset:47104\n"
      "v_mfma_f32_16x16x32_bf16 v[236:239], v[248:251], v[176:179], v[236:239]\n"
      "s_waitcnt lgkmcnt(2)\n"
      "v_mfma_f32_16x16x32_bf16 v[224:227], v[240:243], v[12:15], v[224:227]\n"
      "ds_read_b64_tr_b16 v[248:249], v253 offset:49152\n"
      "v_mfma_f32_16x16x32_bf16 v[228:231], v[240:243], v[68:71], v[228:231]\n"
      "v_mfma_f32_16x16x32_bf16 v[232:235], v[240:243], v[124:127], v[232:235]\n"
      "ds_read_b64_tr_b16 v[250:251], v253 offset:51200\n"
      "v_mfma_f32_16x16x32_bf16 v[236:239], v[240:243], v[180:183], v[236:239]\n"
      "s_waitcnt lgkmcnt(2)\n"
      "v_mfma_f32_16x16x32_bf16 v[224:227], v[244:247], v[16:19], v[224:227]\n"
      "ds_read_b64_tr_b16 v[240:241], v253 offset:53248\n"
      "v_mfma_f32_16x16x32_bf16 v[228:231], v[244:247], v[72:75], v[228:231]\n"
      "v_mfma_f32_16x16x32_bf16 v[232:235], v[244:247], v[128:131], v[232:235]\n"
      "ds_read_b64_tr_b16 v[242:243], v253 offset:55296\n"
      "v_mfma_f32_16x16x32_bf16 v[236:239], v[244:247], v[184:187], v[236:239]\n"
      "s_waitcnt lgkmcnt(2)\n"
      "v_mfma_f32_16x16x32_bf16 v[224:227], v[248:251], v[20:23], v[224:227]\n"
      "ds_read_b64_tr_b16 v[244:245], v255 offset:0\n"
      "v_mfma_f32_16x16x32_bf16 v[228:231], v[248:251], v[76:79], v[228:231]\n"
      "v_mfma_f32_16x16x32_bf16 v[232:235], v[248:251], v[132:135], v[232:235]\n"
      "ds_read_b64_tr_b16 v[246:247], v255 offset:2048\n"
      "v_mfma_f32_16x16x32_bf16 v[236:239], v[248:251], v[188:191], v[236:239]\n"
      "s_waitcnt lgkmcnt(2)\n"
      "v_mfma_f32_16x16x32_bf16 v[224:227], v[240:243], v[24:27], v[224:227]\n"
      "ds_read_b64_tr_b16 v[248:249], v255 offset:4096\n"
      "v_mfma_f32_16x16x32_bf16 v[228:231], v[240:243], v[80:83], v[228:231]\n"
      "v_mfma_f32_16x16x32_bf16 v[232:235], v[240:243], v[136:139], v[232:235]\n"
      "ds_read_b64_tr_b16 v[250:251], v255 offset:6144\n"
      "v_mfma_f32_16x16x32_bf16 v[236:239], v[240:243], v[192:195], v[236:239]\n"
      "s_waitcnt lgkmcnt(2)\n"
      "v_mfma_f32_16x16x32_bf16 v[224:227], v[244:247], v[28:31], v[224:227]\n"
      "ds_read_b64_tr_b16 v[240:241], v255 offset:8192\n"
      "v_mfma_f32_16x16x32_bf16 v[228:231], v[244:247], v[84:87], v[228:231]\n"
      "v_mfma_f32_16x16x32_bf16 v[232:235], v[244:247], v[140:143], v[232:235]\n"
      "ds_read_b64_tr_b16 v[242:243], v255 offset:10240\n"
      "v_mfma_f32_16x16x32_bf16 v[236:239], v[244:247], v[196:199], v[236:239]\n"
      "s_waitcnt lgkmcnt(2)\n"
      "v_mfma_f32_16x16x32_bf16 v[224:227], v[248:251], v[32:35], v[224:227]\n"
      "ds_read_b64_tr_b16 v[244:245], v255 offset:12288\n"
      "v_mfma_f32_16x16x32_bf16 v[228:231], v[248:251], v[88:91], v[228:231]\n"
      "v_mfma_f32_16x16x32_bf16 v[232:235], v[248:251], v[144:147], v[232:235]\n"
      "ds_read_b64_tr_b16 v[246:247], v255 offset:14336\n"
      "v_mfma_f32_16x16x32_bf16 v[236:239], v[248:251], v[200:203], v[236:239]\n"
      "s_waitcnt lgkmcnt(2)\n"
      "v_mfma_f32_16x16x32_bf16 v[224:227], v[240:243], v[36:39], v[224:227]\n"
      "ds_read_b64_tr_b16 v[248:249], v255 offset:16384\n"
      "v_mfma_f32_16x16x32_bf16 v[228:231], v[240:243], v[92:95], v[228:231]\n"
      "v_mfma_f32_16x16x32_bf16 v[232:235], v[240:243], v[148:151], v[232:235]\n"
      "ds_read_b64_tr_b16 v[250:251], v255 offset:18432\n"
      "v_mfma_f32_16x16x32_bf16 v[236:239], v[240:243], v[204:207], v[236:239]\n"
      "s_waitcnt lgkmcnt(2)\n"
      "v_mfma_f32_16x16x32_bf16 v[224:227], v[244:247], v[40:43], v[224:227]\n"
      "ds_read_b64_tr_b16 v[240:241], v255 offset:20480\n"
      "v_mfma_f32_16x16x32_bf16 v[228:231], v[244:247], v[96:99], v[228:231]\n"
      "v_mfma_f32_16x16x32_bf16 v[232:235], v[244:247], v[152:155], v[232:235]\n"
      "ds_read_b64_tr_b16 v[242:243], v255 offset:22528\n"
      "v_mfma_f32_16x16x32_bf16 v[236:239], v[244:247], v[208:211], v[236:239]\n"
      "s_waitcnt lgkmcnt(2)\n"
      "v_mfma_f32_16x16x32_bf16 v[224:227], v[248:251], v[44:47], v[224:227]\n"
      "ds_read_b64_tr_b16 v[244:245], v255 offset:24576\n"
      "v_mfma_f32_16x16x32_bf16 v[228:231], v[248:251], v[100:103], v[228:231]\n"
      "v_mfma_f32_16x16x32_bf16 v[232:235], v[248:251], v[156:159], v[232:235]\n"
      "ds_read_b64_tr_b16 v[246:247], v255 offset:26624\n"
      "v_mfma_f32_16x16x32_bf16 v[236:239], v[248:251], v[212:215], v[236:239]\n"
      "s_waitcnt lgkmcnt(2)\n"
      "v_mfma_f32_16x16x32_bf16 v[224:227], v[240:243], v[48:51], v[224:227]\n"
      "ds_read_b64_tr_b16 v[248:249], v254 offset:28672\n"
      "v_mfma_f32_16x16x32_bf16 v[228:231], v[240:243], v[104:107], v[228:231]\n"
      "v_mfma_f32_16x16x32_bf16 v[232:235], v[240:243], v[160:163], v[232:235]\n"
      "ds_read_b64_tr_b16 v[250:251], v254 offset:30720\n"
      "v_mfma_f32_16x16x32_bf16 v[236:239], v[240:243], v[216:219], v[236:239]\n"
      "s_waitcnt lgkmcnt(2)\n"
      "v_mfma_f32_16x16x32_bf16 v[224:227], v[244:247], v[52:55], v[224:227]\n"
      "ds_read_b64_tr_b16 v[240:241], v254 offset:32768\n"
      "v_mfma_f32_16x16x32_bf16 v[228:231], v[244:247], v[108:111], v[228:231]\n"
      "v_mfma_f32_16x16x32_bf16 v[232:235], v[244:247], v[164:167], v[232:235]\n"
      "ds_read_b64_tr_b16 v[242:243], v254 offset:34816\n"
      "v_mfma_f32_16x16x32_bf16 v[236:239], v[244:247], v[220:223], v[236:239]\n"
      "s_nop 2\n"
      "v_cvt_pk_bf16_f32 v224, v224, v225\n"
      "v_cvt_pk_bf16_f32 v225, v226, v227\n"
      "v_cvt_pk_bf16_f32 v228, v228, v229\n"
      "v_cvt_pk_bf16_f32 v229, v230, v231\n"
      "ds_write_addtid_b32 v224 offset:35808\n"
      "ds_write_addtid_b32 v225 offset:36072\n"
      "s_waitcnt lgkmcnt(4)\n"
      "v_mfma_f32_16x16x32_bf16 v[224:227], v[248:251], v[0:3], 0\n"
      "ds_write_addtid_b32 v228 offset:37984\n"
      "ds_write_addtid_b32 v229 offset:38248\n"
      "v_cvt_pk_bf16_f32 v232, v232, v233\n"
      "v_cvt_pk_bf16_f32 v233, v234, v235\n"
      "v_mfma_f32_16x16x32_bf16 v[228:231], v[248:251], v[56:59], 0\n"
      "ds_write_addtid_b32 v232 offset:40160\n"
      "ds_write_addtid_b32 v233 offset:40424\n"
      "v_cvt_pk_bf16_f32 v236, v236, v237\n"
      "v_cvt_pk_bf16_f32 v237, v238, v239\n"
      "v_mfma_f32_16x16x32_bf16 v[232:235], v[248:251], v[112:115], 0\n"
      "ds_write_addtid_b32 v236 offset:42336\n"
      "ds_write_addtid_b32 v237 offset:42600\n"
      "v_mfma_f32_16x16x32_bf16 v[236:239], v[248:251], v[168:171], 0\n"
      "ds_read_b64_tr_b16 v[244:245], v254 offset:36864\n"
      "ds_read_b64_tr_b16 v[246:247], v254 offset:38912\n"
      "s_waitcnt lgkmcnt(10)\n"
      "v_mfma_f32_16x16x32_bf16 v[224:227], v[240:243], v[4:7], v[224:227]\n"
      "ds_read_b64_tr_b16 v[248:249], v254 offset:40960\n"
      "v_mfma_f32_16x16x32_bf16 v[228:231], v[240:243], v[60:63], v[228:231]\n"
      "s_waitcnt lgkmcnt(3)\n"
      "s_barrier\n"
      "v_mfma_f32_16x16x32_bf16 v[232:235], v[240:243], v[116:119], v[232:235]\n"
      "ds_read_b64_tr_b16 v[250:251], v254 offset:43008\n"
      "v_mfma_f32_16x16x32_bf16 v[236:239], v[240:243], v[172:175], v[236:239]\n"
      "s_waitcnt lgkmcnt(2)\n"
      "v_mfma_f32_16x16x32_bf16 v[224:227], v[244:247], v[8:11], v[224:227]\n"
      "ds_read_b64_tr_b16 v[240:241], v254 offset:45056\n"
      "v_mfma_f32_16x16x32_bf16 v[228:231], v[244:247], v[64:67], v[228:231]\n"
      "v_mfma_f32_16x16x32_bf16 v[232:235], v[244:247], v[120:123], v[232:235]\n"
      "ds_read_b64_tr_b16 v[242:243], v254 offset:47104\n"
      "v_mfma_f32_16x16x32_bf16 v[236:239], v[244:247], v[176:179], v[236:239]\n"
      "s_waitcnt lgkmcnt(2)\n"
      "v_mfma_f32_16x16x32_bf16 v[224:227], v[248:251], v[12:15], v[224:227]\n"
      "ds_read_b64_tr_b16 v[244:245], v254 offset:49152\n"
      "v_mfma_f32_16x16x32_bf16 v[228:231], v[248:251], v[68:71], v[228:231]\n"
      "v_mfma_f32_16x16x32_bf16 v[232:235], v[248:251], v[124:127], v[232:235]\n"
      "ds_read_b64_tr_b16 v[246:247], v254 offset:51200\n"
      "v_mfma_f32_16x16x32_bf16 v[236:239], v[248:251], v[180:183], v[236:239]\n"
      "s_waitcnt lgkmcnt(2)\n"
      "v_mfma_f32_16x16x32_bf16 v[224:227], v[240:243], v[16:19], v[224:227]\n"
      "ds_read_b64_tr_b16 v[248:249], v254 offset:53248\n"
      "v_mfma_f32_16x16x32_bf16 v[228:231], v[240:243], v[72:75], v[228:231]\n"
      "v_mfma_f32_16x16x32_bf16 v[232:235], v[240:243], v[128:131], v[232:235]\n"
      "ds_read_b64_tr_b16 v[250:251], v254 offset:55296\n"
      "v_mfma_f32_16x16x32_bf16 v[236:239], v[240:243], v[184:187], v[236:239]\n"
      "s_waitcnt lgkmcnt(2)\n"
      "v_mfma_f32_16x16x32_bf16 v[224:227], v[244:247], v[20:23], v[224:227]\n"
      "ds_read_b64_tr_b16 v[240:241], v252 offset:0\n"
      "v_mfma_f32_16x16x32_bf16 v[228:231], v[244:247], v[76:79], v[228:231]\n"
      "v_mfma_f32_16x16x32_bf16 v[232:235], v[244:247], v[132:135], v[232:235]\n"
      "ds_read_b64_tr_b16 v[242:243], v252 offset:2048\n"
      "v_mfma_f32_16x16x32_bf16 v[236:239], v[244:247], v[188:191], v[236:239]\n"
      "s_waitcnt lgkmcnt(2)\n"
      "v_mfma_f32_16x16x32_bf16 v[224:227], v[248:251], v[24:27], v[224:227]\n"
      "ds_read_b64_tr_b16 v[244:245], v252 offset:4096\n"
      "v_mfma_f32_16x16x32_bf16 v[228:231], v[248:251], v[80:83], v[228:231]\n"
      "v_mfma_f32_16x16x32_bf16 v[232:235], v[248:251], v[136:139], v[232:235]\n"
      "ds_read_b64_tr_b16 v[246:247], v252 offset:6144\n"
      "v_mfma_f32_16x16x32_bf16 v[236:239], v[248:251], v[192:195], v[236:239]\n"
      "s_waitcnt lgkmcnt(2)\n"
      "v_mfma_f32_16x16x32_bf16 v[224:227], v[240:243], v[28:31], v[224:227]\n"
      "ds_read_b64_tr_b16 v[248:249], v252 offset:8192\n"
      "v_mfma_f32_16x16x32_bf16 v[228:231], v[240:243], v[84:87], v[228:231]\n"
      "v_mfma_f32_16x16x32_bf16 v[232:235], v[240:243], v[140:143], v[232:235]\n"
      "ds_read_b64_tr_b16 v[250:251], v252 offset:10240\n"
      "v_mfma_f32_16x16x32_bf16 v[236:239], v[240:243], v[196:199], v[236:239]\n"
      "s_waitcnt lgkmcnt(2)\n"
      "v_mfma_f32_16x16x32_bf16 v[224:227], v[244:247], v[32:35], v[224:227]\n"
      "ds_read_b64_tr_b16 v[240:241], v252 offset:12288\n"
      "v_mfma_f32_16x16x32_bf16 v[228:231], v[244:247], v[88:91], v[228:231]\n"
      "v_mfma_f32_16x16x32_bf16 v[232:235], v[244:247], v[144:147], v[232:235]\n"
      "ds_read_b64_tr_b16 v[242:243], v252 offset:14336\n"
      "v_mfma_f32_16x16x32_bf16 v[236:239], v[244:247], v[200:203], v[236:239]\n"
      "s_waitcnt lgkmcnt(2)\n"
      "v_mfma_f32_16x16x32_bf16 v[224:227], v[248:251], v[36:39], v[224:227]\n"
      "ds_read_b64_tr_b16 v[244:245], v252 offset:16384\n"
      "v_mfma_f32_16x16x32_bf16 v[228:231], v[248:251], v[92:95], v[228:231]\n"
      "v_mfma_f32_16x16x32_bf16 v[232:235], v[248:251], v[148:151], v[232:235]\n"
      "ds_read_b64_tr_b16 v[246:247], v252 offset:18432\n"
      "v_mfma_f32_16x16x32_bf16 v[236:239], v[248:251], v[204:207], v[236:239]\n"
      "s_waitcnt lgkmcnt(2)\n"
      "v_mfma_f32_16x16x32_bf16 v[224:227], v[240:243], v[40:43], v[224:227]\n"
      "ds_read_b64_tr_b16 v[248:249], v252 offset:20480\n"
      "v_mfma_f32_16x16x32_bf16 v[228:231], v[240:243], v[96:99], v[228:231]\n"
      "v_mfma_f32_16x16x32_bf16 v[232:235], v[240:243], v[152:155], v[232:235]\n"
      "ds_read_b64_tr_b16 v[250:251], v252 offset:22528\n"
      "v_mfma_f32_16x16x32_bf16 v[236:239], v[240:243], v[208:211], v[236:239]\n"
      "s_waitcnt lgkmcnt(2)\n"
      "v_mfma_f32_16x16x32_bf16 v[224:227], v[244:247], v[44:47], v[224:227]\n"
      "ds_read_b64_tr_b16 v[240:241], v252 offset:24576\n"
      "v_mfma_f32_16x16x32_bf16 v[228:231], v[244:247], v[100:103], v[228:231]\n"
      "v_mfma_f32_16x16x32_bf16 v[232:235], v[244:247], v[156:159], v[232:235]\n"
      "ds_read_b64_tr_b16 v[242:243], v252 offset:26624\n"
      "v_mfma_f32_16x16x32_bf16 v[236:239], v[244:247], v[212:215], v[236:239]\n"
      "s_waitcnt lgkmcnt(2)\n"
      "v_mfma_f32_16x16x32_bf16 v[224:227], v[248:251], v[48:51], v[224:227]\n"
      "ds_read_b64_tr_b16 v[244:245], v255 offset:28672\n"
      "v_mfma_f32_16x16x32_bf16 v[228:231], v[248:251], v[104:107], v[228:231]\n"
      "v_mfma_f32_16x16x32_bf16 v[232:235], v[248:251], v[160:163], v[232:235]\n"
      "ds_read_b64_tr_b16 v[246:247], v255 offset:30720\n"
      "v_mfma_f32_16x16x32_bf16 v[236:239], v[248:251], v[216:219], v[236:239]\n"
      "s_waitcnt lgkmcnt(2)\n"
      "v_mfma_f32_16x16x32_bf16 v[224:227], v[240:243], v[52:55], v[224:227]\n"
      "ds_read_b64_tr_b16 v[248:249], v255 offset:32768\n"
      "v_mfma_f32_16x16x32_bf16 v[228:231], v[240:243], v[108:111], v[228:231]\n"
      "v_mfma_f32_16x16x32_bf16 v[232:235], v[240:243], v[164:167], v[232:235]\n"
      "ds_read_b64_tr_b16 v[250:251], v255 offset:34816\n"
      "v_mfma_f32_16x16x32_bf16 v[236:239], v[240:243], v[220:223], v[236:239]\n"
      "s_nop 2\n"
      "v_cvt_pk_bf16_f32 v224, v224, v225\n"
      "v_cvt_pk_bf16_f32 v225, v226, v227\n"
      "v_cvt_pk_bf16_f32 v228, v228, v229\n"
      "v_cvt_pk_bf16_f32 v229, v230, v231\n"
      "ds_write_addtid_b32 v224 offset:17344\n"
      "ds_write_addtid_b32 v225 offset:17608\n"
      "s_waitcnt lgkmcnt(4)\n"
      "v_mfma_f32_16x16x32_bf16 v[224:227], v[244:247], v[0:3], 0\n"
      "ds_write_addtid_b32 v228 offset:19520\n"
      "ds_write_addtid_b32 v229 offset:19784\n"
      "v_cvt_pk_bf16_f32 v232, v232, v233\n"
      "v_cvt_pk_bf16_f32 v233, v234, v235\n"
      "v_mfma_f32_16x16x32_bf16 v[228:231], v[244:247], v[56:59], 0\n"
      "ds_write_addtid_b32 v232 offset:21696\n"
      "ds_write_addtid_b32 v233 offset:21960\n"
      "v_cvt_pk_bf16_f32 v236, v236, v237\n"
      "v_cvt_pk_bf16_f32 v237, v238, v239\n"
      "v_mfma_f32_16x16x32_bf16 v[232:235], v[244:247], v[112:115], 0\n"
      "ds_write_addtid_b32 v236 offset:23872\n"
      "ds_write_addtid_b32 v237 offset:24136\n"
      "v_mfma_f32_16x16x32_bf16 v[236:239], v[244:247], v[168:171], 0\n"
      "ds_read_b64_tr_b16 v[240:241], v255 offset:36864\n"
      "ds_read_b64_tr_b16 v[242:243], v255 offset:38912\n"
      "s_waitcnt lgkmcnt(10)\n"
      "v_mfma_f32_16x16x32_bf16 v[224:227], v[248:251], v[4:7], v[224:227]\n"
      "ds_read_b64_tr_b16 v[244:245], v255 offset:40960\n"
      "v_mfma_f32_16x16x32_bf16 v[228:231], v[248:251], v[60:63], v[228:231]\n"
      "s_waitcnt lgkmcnt(3)\n"
      "s_barrier\n"
      "v_mfma_f32_16x16x32_bf16 v[232:235], v[248:251], v[116:119], v[232:235]\n"
      "ds_read_b64_tr_b16 v[246:247], v255 offset:43008\n"
      "v_mfma_f32_16x16x32_bf16 v[236:239], v[248:251], v[172:175], v[236:239]\n"
      "s_waitcnt lgkmcnt(2)\n"
      "v_mfma_f32_16x16x32_bf16 v[224:227], v[240:243], v[8:11], v[224:227]\n"
      "ds_read_b64_tr_b16 v[248:249], v255 offset:45056\n"
      "v_mfma_f32_16x16x32_bf16 v[228:231], v[240:243], v[64:67], v[228:231]\n"
      "v_mfma_f32_16x16x32_bf16 v[232:235], v[240:243], v[120:123], v[232:235]\n"
      "ds_read_b64_tr_b16 v[250:251], v255 offset:47104\n"
      "v_mfma_f32_16x16x32_bf16 v[236:239], v[240:243], v[176:179], v[236:239]\n"
      "s_waitcnt lgkmcnt(2)\n"
      "v_mfma_f32_16x16x32_bf16 v[224:227], v[244:247], v[12:15], v[224:227]\n"
      "ds_read_b64_tr_b16 v[240:241], v255 offset:49152\n"
      "v_mfma_f32_16x16x32_bf16 v[228:231], v[244:247], v[68:71], v[228:231]\n"
      "v_mfma_f32_16x16x32_bf16 v[232:235], v[244:247], v[124:127], v[232:235]\n"
      "ds_read_b64_tr_b16 v[242:243], v255 offset:51200\n"
      "v_mfma_f32_16x16x32_bf16 v[236:239], v[244:247], v[180:183], v[236:239]\n"
      "s_waitcnt lgkmcnt(2)\n"
      "v_mfma_f32_16x16x32_bf16 v[224:227], v[248:251], v[16:19], v[224:227]\n"
      "ds_read_b64_tr_b16 v[244:245], v255 offset:53248\n"
      "v_mfma_f32_16x16x32_bf16 v[228:231], v[248:251], v[72:75], v[228:231]\n"
      "v_mfma_f32_16x16x32_bf16 v[232:235], v[248:251], v[128:131], v[232:235]\n"
      "ds_read_b64_tr_b16 v[246:247], v255 offset:55296\n"
      "v_mfma_f32_16x16x32_bf16 v[236:239], v[248:251], v[184:187], v[236:239]\n"
      "s_waitcnt lgkmcnt(2)\n"
      "v_mfma_f32_16x16x32_bf16 v[224:227], v[240:243], v[20:23], v[224:227]\n"
      "ds_read_b64_tr_b16 v[248:249], v253 offset:0\n"
      "v_mfma_f32_16x16x32_bf16 v[228:231], v[240:243], v[76:79], v[228:231]\n"
      "v_mfma_f32_16x16x32_bf16 v[232:235], v[240:243], v[132:135], v[232:235]\n"
      "ds_read_b64_tr_b16 v[250:251], v253 offset:2048\n"
      "v_mfma_f32_16x16x32_bf16 v[236:239], v[240:243], v[188:191], v[236:239]\n"
      "s_waitcnt lgkmcnt(2)\n"
      "v_mfma_f32_16x16x32_bf16 v[224:227], v[244:247], v[24:27], v[224:227]\n"
      "ds_read_b64_tr_b16 v[240:241], v253 offset:4096\n"
      "v_mfma_f32_16x16x32_bf16 v[228:231], v[244:247], v[80:83], v[228:231]\n"
      "v_mfma_f32_16x16x32_bf16 v[232:235], v[244:247], v[136:139], v[232:235]\n"
      "ds_read_b64_tr_b16 v[242:243], v253 offset:6144\n"
      "v_mfma_f32_16x16x32_bf16 v[236:239], v[244:247], v[192:195], v[236:239]\n"
      "s_waitcnt lgkmcnt(2)\n"
      "v_mfma_f32_16x16x32_bf16 v[224:227], v[248:251], v[28:31], v[224:227]\n"
      "ds_read_b64_tr_b16 v[244:245], v253 offset:8192\n"
      "v_mfma_f32_16x16x32_bf16 v[228:231], v[248:251], v[84:87], v[228:231]\n"
      "v_mfma_f32_16x16x32_bf16 v[232:235], v[248:251], v[140:143], v[232:235]\n"
      "ds_read_b64_tr_b16 v[246:247], v253 offset:10240\n"
      "v_mfma_f32_16x16x32_bf16 v[236:239], v[248:251], v[196:199], v[236:239]\n"
      "s_waitcnt lgkmcnt(2)\n"
      "v_mfma_f32_16x16x32_bf16 v[224:227], v[240:243], v[32:35], v[224:227]\n"
      "ds_read_b64_tr_b16 v[248:249], v253 offset:12288\n"
      "v_mfma_f32_16x16x32_bf16 v[228:231], v[240:243], v[88:91], v[228:231]\n"
      "v_mfma_f32_16x16x32_bf16 v[232:235], v[240:243], v[144:147], v[232:235]\n"
      "ds_read_b64_tr_b16 v[250:251], v253 offset:14336\n"
      "v_mfma_f32_16x16x32_bf16 v[236:239], v[240:243], v[200:203], v[236:239]\n"
      "s_waitcnt lgkmcnt(2)\n"
      "v_mfma_f32_16x16x32_bf16 v[224:227], v[244:247], v[36:39], v[224:227]\n"
      "ds_read_b64_tr_b16 v[240:241], v253 offset:16384\n"
      "v_mfma_f32_16x16x32_bf16 v[228:231], v[244:247], v[92:95], v[228:231]\n"
      "v_mfma_f32_16x16x32_bf16 v[232:235], v[244:247], v[148:151], v[232:235]\n"
      "ds_read_b64_tr_b16 v[242:243], v253 offset:18432\n"
      "v_mfma_f32_16x16x32_bf16 v[236:239], v[244:247], v[204:207], v[236:239]\n"
      "s_waitcnt lgkmcnt(2)\n"
      "v_mfma_f32_16x16x32_bf16 v[224:227], v[248:251], v[40:43], v[224:227]\n"
      "ds_read_b64_tr_b16 v[244:245], v253 offset:20480\n"
      "v_mfma_f32_16x16x32_bf16 v[228:231], v[248:251], v[96:99], v[228:231]\n"
      "v_mfma_f32_16x16x32_bf16 v[232:235], v[248:251], v[152:155], v[232:235]\n"
      "ds_read_b64_tr_b16 v[246:247], v253 offset:22528\n"
      "v_mfma_f32_16x16x32_bf16 v[236:239], v[248:251], v[208:211], v[236:239]\n"
      "s_waitcnt lgkmcnt(2)\n"
      "v_mfma_f32_16x16x32_bf16 v[224:227], v[240:243], v[44:47], v[224:227]\n"
      "ds_read_b64_tr_b16 v[248:249], v253 offset:24576\n"
      "v_mfma_f32_16x16x32_bf16 v[228:231], v[240:243], v[100:103], v[228:231]\n"
      "v_mfma_f32_16x16x32_bf16 v[232:235], v[240:243], v[156:159], v[232:235]\n"
      "ds_read_b64_tr_b16 v[250:251], v253 offset:26624\n"
      "v_mfma_f32_16x16x32_bf16 v[236:239], v[240:243], v[212:215], v[236:239]\n"
      "s_waitcnt lgkmcnt(2)\n"
      "v_mfma_f32_16x16x32_bf16 v[224:227], v[244:247], v[48:51], v[224:227]\n"
      "ds_read_b64_tr_b16 v[240:241], v252 offset:28672\n"
      "v_mfma_f32_16x16x32_bf16 v[228:231], v[244:247], v[104:107], v[228:231]\n"
      "v_mfma_f32_16x16x32_bf16 v[232:235], v[244:247], v[160:163], v[232:235]\n"
      "ds_read_b64_tr_b16 v[242:243], v252 offset:30720\n"
      "v_mfma_f32_16x16x32_bf16 v[236:239], v[244:247], v[216:219], v[236:239]\n"
      "s_waitcnt lgkmcnt(2)\n"
      "v_mfma_f32_16x16x32_bf16 v[224:227], v[248:251], v[52:55], v[224:227]\n"
      "ds_read_b64_tr_b16 v[244:245], v252 offset:32768\n"
      "v_mfma_f32_16x16x32_bf16 v[228:231], v[248:251], v[108:111], v[228:231]\n"
      "v_mfma_f32_16x16x32_bf16 v[232:235], v[248:251], v[164:167], v[232:235]\n"
      "ds_read_b64_tr_b16 v[246:247], v252 offset:34816\n"
      "v_mfma_f32_16x16x32_bf16 v[236:239], v[248:251], v[220:223], v[236:239]\n"
      "s_nop 2\n"
      "v_cvt_pk_bf16_f32 v224, v224, v225\n"
      "v_cvt_pk_bf16_f32 v225, v226, v227\n"
      "v_cvt_pk_bf16_f32 v228, v228, v229\n"
      "v_cvt_pk_bf16_f32 v229, v230, v231\n"
      "ds_write_addtid_b32 v224 offset:18400\n"
      "ds_write_addtid_b32 v225 offset:18664\n"
      "s_waitcnt lgkmcnt(4)\n"
      "v_mfma_f32_16x16x32_bf16 v[224:227], v[240:243], v[0:3], 0\n"
      "ds_write_addtid_b32 v228 offset:20576\n"
      "ds_write_addtid_b32 v229 offset:20840\n"
      "v_cvt_pk_bf16_f32 v232, v232, v233\n"
      "v_cvt_pk_bf16_f32 v233, v234, v235\n"
      "v_mfma_f32_16x16x32_bf16 v[228:231], v[240:243], v[56:59], 0\n"
      "ds_write_addtid_b32 v232 offset:22752\n"
      "ds_write_addtid_b32 v233 offset:23016\n"
      "v_cvt_pk_bf16_f32 v236, v236, v237\n"
      "v_cvt_pk_bf16_f32 v237, v238, v239\n"
      "v_mfma_f32_16x16x32_bf16 v[232:235], v[240:243], v[112:115], 0\n"
      "ds_write_addtid_b32 v236 offset:24928\n"
      "ds_write_addtid_b32 v237 offset:25192\n"
      "v_mfma_f32_16x16x32_bf16 v[236:239], v[240:243], v[168:171], 0\n"
      "ds_read_b64_tr_b16 v[248:249], v252 offset:36864\n"
      "ds_read_b64_tr_b16 v[250:251], v252 offset:38912\n"
      "s_waitcnt lgkmcnt(10)\n"
      "v_mfma_f32_16x16x32_bf16 v[224:227], v[244:247], v[4:7], v[224:227]\n"
      "ds_read_b64_tr_b16 v[240:241], v252 offset:40960\n"
      "v_mfma_f32_16x16x32_bf16 v[228:231], v[244:247], v[60:63], v[228:231]\n"
      "s_waitcnt lgkmcnt(3)\n"
      "s_barrier\n"
      "v_mfma_f32_16x16x32_bf16 v[232:235], v[244:247], v[116:119], v[232:235]\n"
      "ds_read_b64_tr_b16 v[242:243], v252 offset:43008\n"
      "v_mfma_f32_16x16x32_bf16 v[236:239], v[244:247], v[172:175], v[236:239]\n"
      "s_waitcnt lgkmcnt(2)\n"
      "v_mfma_f32_16x16x32_bf16 v[224:227], v[248:251], v[8:11], v[224:227]\n"
      "ds_read_b64_tr_b16 v[244:245], v252 offset:45056\n"
      "v_mfma_f32_16x16x32_bf16 v[228:231], v[248:251], v[64:67], v[228:231]\n"
      "v_mfma_f32_16x16x32_bf16 v[232:235], v[248:251], v[120:123], v[232:235]\n"
      "ds_read_b64_tr_b16 v[246:247], v252 offset:47104\n"
      "v_mfma_f32_16x16x32_bf16 v[236:239], v[248:251], v[176:179], v[236:239]\n"
      "s_waitcnt lgkmcnt(2)\n"
      "v_mfma_f32_16x16x32_bf16 v[224:227], v[240:243], v[12:15], v[224:227]\n"
      "ds_read_b64_tr_b16 v[248:249], v252 offset:49152\n"
      "v_mfma_f32_16x16x32_bf16 v[228:231], v[240:243], v[68:71], v[228:231]\n"
      "v_mfma_f32_16x16x32_bf16 v[232:235], v[240:243], v[124:127], v[232:235]\n"
      "ds_read_b64_tr_b16 v[250:251], v252 offset:51200\n"
      "v_mfma_f32_16x16x32_bf16 v[236:239], v[240:243], v[180:183], v[236:239]\n"
      "s_waitcnt lgkmcnt(2)\n"
      "v_mfma_f32_16x16x32_bf16 v[224:227], v[244:247], v[16:19], v[224:227]\n"
      "ds_read_b64_tr_b16 v[240:241], v252 offset:53248\n"
      "v_mfma_f32_16x16x32_bf16 v[228:231], v[244:247], v[72:75], v[228:231]\n"
      "v_mfma_f32_16x16x32_bf16 v[232:235], v[244:247], v[128:131], v[232:235]\n"
      "ds_read_b64_tr_b16 v[242:243], v252 offset:55296\n"
      "v_mfma_f32_16x16x32_bf16 v[236:239], v[244:247], v[184:187], v[236:239]\n"
      "s_waitcnt lgkmcnt(2)\n"
      "v_mfma_f32_16x16x32_bf16 v[224:227], v[248:251], v[20:23], v[224:227]\n"
      "ds_read_b64_tr_b16 v[244:245], v254 offset:0\n"
      "v_mfma_f32_16x16x32_bf16 v[228:231], v[248:251], v[76:79], v[228:231]\n"
      "v_mfma_f32_16x16x32_bf16 v[232:235], v[248:251], v[132:135], v[232:235]\n"
      "ds_read_b64_tr_b16 v[246:247], v254 offset:2048\n"
      "v_mfma_f32_16x16x32_bf16 v[236:239], v[248:251], v[188:191], v[236:239]\n"
      "s_waitcnt lgkmcnt(2)\n"
      "v_mfma_f32_16x16x32_bf16 v[224:227], v[240:243], v[24:27], v[224:227]\n"
      "ds_read_b64_tr_b16 v[248:249], v254 offset:4096\n"
      "v_mfma_f32_16x16x32_bf16 v[228:231], v[240:243], v[80:83], v[228:231]\n"
      "v_mfma_f32_16x16x32_bf16 v[232:235], v[240:243], v[136:139], v[232:235]\n"
      "ds_read_b64_tr_b16 v[250:251], v254 offset:6144\n"
      "v_mfma_f32_16x16x32_bf16 v[236:239], v[240:243], v[192:195], v[236:239]\n"
      "s_waitcnt lgkmcnt(2)\n"
      "v_mfma_f32_16x16x32_bf16 v[224:227], v[244:247], v[28:31], v[224:227]\n"
      "ds_read_b64_tr_b16 v[240:241], v254 offset:8192\n"
      "v_mfma_f32_16x16x32_bf16 v[228:231], v[244:247], v[84:87], v[228:231]\n"
      "v_mfma_f32_16x16x32_bf16 v[232:235], v[244:247], v[140:143], v[232:235]\n"
      "ds_read_b64_tr_b16 v[242:243], v254 offset:10240\n"
      "v_mfma_f32_16x16x32_bf16 v[236:239], v[244:247], v[196:199], v[236:239]\n"
      "s_waitcnt lgkmcnt(2)\n"
      "v_mfma_f32_16x16x32_bf16 v[224:227], v[248:251], v[32:35], v[224:227]\n"
      "ds_read_b64_tr_b16 v[244:245], v254 offset:12288\n"
      "v_mfma_f32_16x16x32_bf16 v[228:231], v[248:251], v[88:91], v[228:231]\n"
      "v_mfma_f32_16x16x32_bf16 v[232:235], v[248:251], v[144:147], v[232:235]\n"
      "ds_read_b64_tr_b16 v[246:247], v254 offset:14336\n"
      "v_mfma_f32_16x16x32_bf16 v[236:239], v[248:251], v[200:203], v[236:239]\n"
      "s_waitcnt lgkmcnt(2)\n"
      "v_mfma_f32_16x16x32_bf16 v[224:227], v[240:243], v[36:39], v[224:227]\n"
      "ds_read_b64_tr_b16 v[248:249], v254 offset:16384\n"
      "v_mfma_f32_16x16x32_bf16 v[228:231], v[240:243], v[92:95], v[228:231]\n"
      "v_mfma_f32_16x16x32_bf16 v[232:235], v[240:243], v[148:151], v[232:235]\n"
      "ds_read_b64_tr_b16 v[250:251], v254 offset:18432\n"
      "v_mfma_f32_16x16x32_bf16 v[236:239], v[240:243], v[204:207], v[236:239]\n"
      "s_waitcnt lgkmcnt(2)\n"
      "v_mfma_f32_16x16x32_bf16 v[224:227], v[244:247], v[40:43], v[224:227]\n"
      "ds_read_b64_tr_b16 v[240:241], v254 offset:20480\n"
      "v_mfma_f32_16x16x32_bf16 v[228:231], v[244:247], v[96:99], v[228:231]\n"
      "v_mfma_f32_16x16x32_bf16 v[232:235], v[244:247], v[152:155], v[232:235]\n"
      "ds_read_b64_tr_b16 v[242:243], v254 offset:22528\n"
      "v_mfma_f32_16x16x32_bf16 v[236:239], v[244:247], v[208:211], v[236:239]\n"
      "s_waitcnt lgkmcnt(2)\n"
      "v_mfma_f32_16x16x32_bf16 v[224:227], v[248:251], v[44:47], v[224:227]\n"
      "ds_read_b64_tr_b16 v[244:245], v254 offset:24576\n"
      "v_mfma_f32_16x16x32_bf16 v[228:231], v[248:251], v[100:103], v[228:231]\n"
      "v_mfma_f32_16x16x32_bf16 v[232:235], v[248:251], v[156:159], v[232:235]\n"
      "ds_read_b64_tr_b16 v[246:247], v254 offset:26624\n"
      "v_mfma_f32_16x16x32_bf16 v[236:239], v[248:251], v[212:215], v[236:239]\n"
      "s_waitcnt lgkmcnt(2)\n"
      "v_mfma_f32_16x16x32_bf16 v[224:227], v[240:243], v[48:51], v[224:227]\n"
      "ds_read_b64_tr_b16 v[248:249], v253 offset:28672\n"
      "v_mfma_f32_16x16x32_bf16 v[228:231], v[240:243], v[104:107], v[228:231]\n"
      "v_mfma_f32_16x16x32_bf16 v[232:235], v[240:243], v[160:163], v[232:235]\n"
      "ds_read_b64_tr_b16 v[250:251], v253 offset:30720\n"
      "v_mfma_f32_16x16x32_bf16 v[236:239], v[240:243], v[216:219], v[236:239]\n"
      "s_waitcnt lgkmcnt(2)\n"
      "v_mfma_f32_16x16x32_bf16 v[224:227], v[244:247], v[52:55], v[224:227]\n"
      "ds_read_b64_tr_b16 v[240:241], v253 offset:32768\n"
      "v_mfma_f32_16x16x32_bf16 v[228:231], v[244:247], v[108:111], v[228:231]\n"
      "v_mfma_f32_16x16x32_bf16 v[232:235], v[244:247], v[164:167], v[232:235]\n"
      "ds_read_b64_tr_b16 v[242:243], v253 offset:34816\n"
      "v_mfma_f32_16x16x32_bf16 v[236:239], v[244:247], v[220:223], v[236:239]\n"
      "s_nop 2\n"
      "v_cvt_pk_bf16_f32 v224, v224, v225\n"
      "v_cvt_pk_bf16_f32 v225, v226, v227\n"
      "v_cvt_pk_bf16_f32 v228, v228, v229\n"
      "v_cvt_pk_bf16_f32 v229, v230, v231\n"
      "ds_write_addtid_b32 v224 offset:34752\n"
      "ds_write_addtid_b32 v225 offset:35016\n"
      "s_waitcnt lgkmcnt(4)\n"
      "v_mfma_f32_16x16x32_bf16 v[224:227], v[248:251], v[0:3], 0\n"
      "ds_write_addtid_b32 v228 offset:36928\n"
      "ds_write_addtid_b32 v229 offset:37192\n"
      "v_cvt_pk_bf16_f32 v232, v232, v233\n"
      "v_cvt_pk_bf16_f32 v233, v234, v235\n"
      "v_mfma_f32_16x16x32_bf16 v[228:231], v[248:251], v[56:59], 0\n"
      "ds_write_addtid_b32 v232 offset:39104\n"
      "ds_write_addtid_b32 v233 offset:39368\n"
      "v_cvt_pk_bf16_f32 v236, v236, v237\n"
      "v_cvt_pk_bf16_f32 v237, v238, v239\n"
      "v_mfma_f32_16x16x32_bf16 v[232:235], v[248:251], v[112:115], 0\n"
      "ds_write_addtid_b32 v236 offset:41280\n"
      "ds_write_addtid_b32 v237 offset:41544\n"
      "v_mfma_f32_16x16x32_bf16 v[236:239], v[248:251], v[168:171], 0\n"
      "ds_read_b64_tr_b16 v[244:245], v253 offset:36864\n"
      "ds_read_b64_tr_b16 v[246:247], v253 offset:38912\n"
      "s_waitcnt lgkmcnt(10)\n"
      "v_mfma_f32_16x16x32_bf16 v[224:227], v[240:243], v[4:7], v[224:227]\n"
      "ds_read_b64_tr_b16 v[248:249], v253 offset:40960\n"
      "v_mfma_f32_16x16x32_bf16 v[228:231], v[240:243], v[60:63], v[228:231]\n"
      "s_waitcnt lgkmcnt(3)\n"
      "s_barrier\n"
      "v_mfma_f32_16x16x32_bf16 v[232:235], v[240:243], v[116:119], v[232:235]\n"
      "ds_read_b64_tr_b16 v[250:251], v253 offset:43008\n"
      "v_mfma_f32_16x16x32_bf16 v[236:239], v[240:243], v[172:175], v[236:239]\n"
      "s_waitcnt lgkmcnt(2)\n"
      "v_mfma_f32_16x16x32_bf16 v[224:227], v[244:247], v[8:11], v[224:227]\n"
      "ds_read_b64_tr_b16 v[240:241], v253 offset:45056\n"
      "v_mfma_f32_16x16x32_bf16 v[228:231], v[244:247], v[64:67], v[228:231]\n"
      "v_mfma_f32_16x16x32_bf16 v[232:235], v[244:247], v[120:123], v[232:235]\n"
      "ds_read_b64_tr_b16 v[242:243], v253 offset:47104\n"
      "v_mfma_f32_16x16x32_bf16 v[236:239], v[244:247], v[176:179], v[236:239]\n"
      "s_waitcnt lgkmcnt(2)\n"
      "v_mfma_f32_16x16x32_bf16 v[224:227], v[248:251], v[12:15], v[224:227]\n"
      "ds_read_b64_tr_b16 v[244:245], v253 offset:49152\n"
      "v_mfma_f32_16x16x32_bf16 v[228:231], v[248:251], v[68:71], v[228:231]\n"
      "v_mfma_f32_16x16x32_bf16 v[232:235], v[248:251], v[124:127], v[232:235]\n"
      "ds_read_b64_tr_b16 v[246:247], v253 offset:51200\n"
      "v_mfma_f32_16x16x32_bf16 v[236:239], v[248:251], v[180:183], v[236:239]\n"
      "s_waitcnt lgkmcnt(2)\n"
      "v_mfma_f32_16x16x32_bf16 v[224:227], v[240:243], v[16:19], v[224:227]\n"
      "ds_read_b64_tr_b16 v[248:249], v253 offset:53248\n"
      "v_mfma_f32_16x16x32_bf16 v[228:231], v[240:243], v[72:75], v[228:231]\n"
      "v_mfma_f32_16x16x32_bf16 v[232:235], v[240:243], v[128:131], v[232:235]\n"
      "ds_read_b64_tr_b16 v[250:251], v253 offset:55296\n"
      "v_mfma_f32_16x16x32_bf16 v[236:239], v[240:243], v[184:187], v[236:239]\n"
      "s_waitcnt lgkmcnt(2)\n"
      "v_mfma_f32_16x16x32_bf16 v[224:227], v[244:247], v[20:23], v[224:227]\n"
      "ds_read_b64_tr_b16 v[240:241], v255 offset:0\n"
      "v_mfma_f32_16x16x32_bf16 v[228:231], v[244:247], v[76:79], v[228:231]\n"
      "v_mfma_f32_16x16x32_bf16 v[232:235], v[244:247], v[132:135], v[232:235]\n"
      "ds_read_b64_tr_b16 v[242:243], v255 offset:2048\n"
      "v_mfma_f32_16x16x32_bf16 v[236:239], v[244:247], v[188:191], v[236:239]\n"
      "s_waitcnt lgkmcnt(2)\n"
      "v_mfma_f32_16x16x32_bf16 v[224:227], v[248:251], v[24:27], v[224:227]\n"
      "ds_read_b64_tr_b16 v[244:245], v255 offset:4096\n"
      "v_mfma_f32_16x16x32_bf16 v[228:231], v[248:251], v[80:83], v[228:231]\n"
      "v_mfma_f32_16x16x32_bf16 v[232:235], v[248:251], v[136:139], v[232:235]\n"
      "ds_read_b64_tr_b16 v[246:247], v255 offset:6144\n"
      "v_mfma_f32_16x16x32_bf16 v[236:239], v[248:251], v[192:195], v[236:239]\n"
      "s_waitcnt lgkmcnt(2)\n"
      "v_mfma_f32_16x16x32_bf16 v[224:227], v[240:243], v[28:31], v[224:227]\n"
      "ds_read_b64_tr_b16 v[248:249], v255 offset:8192\n"
      "v_mfma_f32_16x16x32_bf16 v[228:231], v[240:243], v[84:87], v[228:231]\n"
      "v_mfma_f32_16x16x32_bf16 v[232:235], v[240:243], v[140:143], v[232:235]\n"
      "ds_read_b64_tr_b16 v[250:251], v255 offset:10240\n"
      "v_mfma_f32_16x16x32_bf16 v[236:239], v[240:243], v[196:199], v[236:239]\n"
      "s_waitcnt lgkmcnt(2)\n"
      "v_mfma_f32_16x16x32_bf16 v[224:227], v[244:247], v[32:35], v[224:227]\n"
      "ds_read_b64_tr_b16 v[240:241], v255 offset:12288\n"
      "v_mfma_f32_16x16x32_bf16 v[228:231], v[244:247], v[88:91], v[228:231]\n"
      "v_mfma_f32_16x16x32_bf16 v[232:235], v[244:247], v[144:147], v[232:235]\n"
      "ds_read_b64_tr_b16 v[242:243], v255 offset:14336\n"
      "v_mfma_f32_16x16x32_bf16 v[236:239], v[244:247], v[200:203], v[236:239]\n"
      "s_waitcnt lgkmcnt(2)\n"
      "v_mfma_f32_16x16x32_bf16 v[224:227], v[248:251], v[36:39], v[224:227]\n"
      "ds_read_b64_tr_b16 v[244:245], v255 offset:16384\n"
      "v_mfma_f32_16x16x32_bf16 v[228:231], v[248:251], v[92:95], v[228:231]\n"
      "v_mfma_f32_16x16x32_bf16 v[232:235], v[248:251], v[148:151], v[232:235]\n"
      "ds_read_b64_tr_b16 v[246:247], v255 offset:18432\n"
      "v_mfma_f32_16x16x32_bf16 v[236:239], v[248:251], v[204:207], v[236:239]\n"
      "s_waitcnt lgkmcnt(2)\n"
      "v_mfma_f32_16x16x32_bf16 v[224:227], v[240:243], v[40:43], v[224:227]\n"
      "ds_read_b64_tr_b16 v[248:249], v255 offset:20480\n"
      "v_mfma_f32_16x16x32_bf16 v[228:231], v[240:243], v[96:99], v[228:231]\n"
      "v_mfma_f32_16x16x32_bf16 v[232:235], v[240:243], v[152:155], v[232:235]\n"
      "ds_read_b64_tr_b16 v[250:251], v255 offset:22528\n"
      "v_mfma_f32_16x16x32_bf16 v[236:239], v[240:243], v[208:211], v[236:239]\n"
      "s_waitcnt lgkmcnt(2)\n"
      "v_mfma_f32_16x16x32_bf16 v[224:227], v[244:247], v[44:47], v[224:227]\n"
      "ds_read_b64_tr_b16 v[240:241], v255 offset:24576\n"
      "v_mfma_f32_16x16x32_bf16 v[228:231], v[244:247], v[100:103], v[228:231]\n"
      "v_mfma_f32_16x16x32_bf16 v[232:235], v[244:247], v[156:159], v[232:235]\n"
      "ds_read_b64_tr_b16 v[242:243], v255 offset:26624\n"
      "v_mfma_f32_16x16x32_bf16 v[236:239], v[244:247], v[212:215], v[236:239]\n"
      "s_waitcnt lgkmcnt(2)\n"
      "v_mfma_f32_16x16x32_bf16 v[224:227], v[248:251], v[48:51], v[224:227]\n"
      "ds_read_b64_tr_b16 v[244:245], v254 offset:28672\n"
      "v_mfma_f32_16x16x32_bf16 v[228:231], v[248:251], v[104:107], v[228:231]\n"
      "v_mfma_f32_16x16x32_bf16 v[232:235], v[248:251], v[160:163], v[232:235]\n"
      "ds_read_b64_tr_b16 v[246:247], v254 offset:30720\n"
      "v_mfma_f32_16x16x32_bf16 v[236:239], v[248:251], v[216:219], v[236:239]\n"
      "s_waitcnt lgkmcnt(2)\n"
      "v_mfma_f32_16x16x32_bf16 v[224:227], v[240:243], v[52:55], v[224:227]\n"
      "ds_read_b64_tr_b16 v[248:249], v254 offset:32768\n"
      "v_mfma_f32_16x16x32_bf16 v[228:231], v[240:243], v[108:111], v[228:231]\n"
      "v_mfma_f32_16x16x32_bf16 v[232:235], v[240:243], v[164:167], v[232:235]\n"
      "ds_read_b64_tr_b16 v[250:251], v254 offset:34816\n"
      "v_mfma_f32_16x16x32_bf16 v[236:239], v[240:243], v[220:223], v[236:239]\n"
      "s_nop 2\n"
      "v_cvt_pk_bf16_f32 v224, v224, v225\n"
      "v_cvt_pk_bf16_f32 v225, v226, v227\n"
      "v_cvt_pk_bf16_f32 v228, v228, v229\n"
      "v_cvt_pk_bf16_f32 v229, v230, v231\n"
      "ds_write_addtid_b32 v224 offset:35808\n"
      "ds_write_addtid_b32 v225 offset:36072\n"
      "s_waitcnt lgkmcnt(4)\n"
      "v_mfma_f32_16x16x32_bf16 v[224:227], v[244:247], v[0:3], 0\n"
      "ds_write_addtid_b32 v228 offset:37984\n"
      "ds_write_addtid_b32 v229 offset:38248\n"
      "v_cvt_pk_bf16_f32 v232, v232, v233\n"
      "v_cvt_pk_bf16_f32 v233, v234, v235\n"
      "v_mfma_f32_16x16x32_bf16 v[228:231], v[244:247], v[56:59], 0\n"
      "ds_write_addtid_b32 v232 offset:40160\n"
      "ds_write_addtid_b32 v233 offset:40424\n"
      "v_cvt_pk_bf16_f32 v236, v236, v237\n"
      "v_cvt_pk_bf16_f32 v237, v238, v239\n"
      "v_mfma_f32_16x16x32_bf16 v[232:235], v[244:247], v[112:115], 0\n"
      "ds_write_addtid_b32 v236 offset:42336\n"
      "ds_write_addtid_b32 v237 offset:42600\n"
      "v_mfma_f32_16x16x32_bf16 v[236:239], v[244:247], v[168:171], 0\n"
      "ds_read_b64_tr_b16 v[240:241], v254 offset:36864\n"
      "ds_read_b64_tr_b16 v[242:243], v254 offset:38912\n"
      "s_waitcnt lgkmcnt(10)\n"
      "v_mfma_f32_16x16x32_bf16 v[224:227], v[248:251], v[4:7], v[224:227]\n"
      "ds_read_b64_tr_b16 v[244:245], v254 offset:40960\n"
      "v_mfma_f32_16x16x32_bf16 v[228:231], v[248:251], v[60:63], v[228:231]\n"
      "s_waitcnt lgkmcnt(3)\n"
      "s_barrier\n"
      "v_mfma_f32_16x16x32_bf16 v[232:235], v[248:251], v[116:119], v[232:235]\n"
      "ds_read_b64_tr_b16 v[246:247], v254 offset:43008\n"
      "v_mfma_f32_16x16x32_bf16 v[236:239], v[248:251], v[172:175], v[236:239]\n"
      "s_waitcnt lgkmcnt(2)\n"
      "v_mfma_f32_16x16x32_bf16 v[224:227], v[240:243], v[8:11], v[224:227]\n"
      "ds_read_b64_tr_b16 v[248:249], v254 offset:45056\n"
      "v_mfma_f32_16x16x32_bf16 v[228:231], v[240:243], v[64:67], v[228:231]\n"
      "v_mfma_f32_16x16x32_bf16 v[232:235], v[240:243], v[120:123], v[232:235]\n"
      "ds_read_b64_tr_b16 v[250:251], v254 offset:47104\n"
      "v_mfma_f32_16x16x32_bf16 v[236:239], v[240:243], v[176:179], v[236:239]\n"
      "s_waitcnt lgkmcnt(2)\n"
      "v_mfma_f32_16x16x32_bf16 v[224:227], v[244:247], v[12:15], v[224:227]\n"
      "ds_read_b64_tr_b16 v[240:241], v254 offset:49152\n"
      "v_mfma_f32_16x16x32_bf16 v[228:231], v[244:247], v[68:71], v[228:231]\n"
      "v_mfma_f32_16x16x32_bf16 v[232:235], v[244:247], v[124:127], v[232:235]\n"
      "ds_read_b64_tr_b16 v[242:243], v254 offset:51200\n"
      "v_mfma_f32_16x16x32_bf16 v[236:239], v[244:247], v[180:183], v[236:239]\n"
      "s_waitcnt lgkmcnt(2)\n"
      "v_mfma_f32_16x16x32_bf16 v[224:227], v[248:251], v[16:19], v[224:227]\n"
      "ds_read_b64_tr_b16 v[244:245], v254 offset:53248\n"
      "v_mfma_f32_16x16x32_bf16 v[228:231], v[248:251], v[72:75], v[228:231]\n"
      "v_mfma_f32_16x16x32_bf16 v[232:235], v[248:251], v[128:131], v[232:235]\n"
      "ds_read_b64_tr_b16 v[246:247], v254 offset:55296\n"
      "v_mfma_f32_16x16x32_bf16 v[236:239], v[248:251], v[184:187], v[236:239]\n"
      "s_waitcnt lgkmcnt(2)\n"
      "v_mfma_f32_16x16x32_bf16 v[224:227], v[240:243], v[20:23], v[224:227]\n"
      "ds_read_b64_tr_b16 v[248:249], v252 offset:0\n"
      "v_mfma_f32_16x16x32_bf16 v[228:231], v[240:243], v[76:79], v[228:231]\n"
      "v_mfma_f32_16x16x32_bf16 v[232:235], v[240:243], v[132:135], v[232:235]\n"
      "ds_read_b64_tr_b16 v[250:251], v252 offset:2048\n"
      "v_mfma_f32_16x16x32_bf16 v[236:239], v[240:243], v[188:191], v[236:239]\n"
      "s_waitcnt lgkmcnt(2)\n"
      "v_mfma_f32_16x16x32_bf16 v[224:227], v[244:247], v[24:27], v[224:227]\n"
      "ds_read_b64_tr_b16 v[240:241], v252 offset:4096\n"
      "v_mfma_f32_16x16x32_bf16 v[228:231], v[244:247], v[80:83], v[228:231]\n"
      "v_mfma_f32_16x16x32_bf16 v[232:235], v[244:247], v[136:139], v[232:235]\n"
      "ds_read_b64_tr_b16 v[242:243], v252 offset:6144\n"
      "v_mfma_f32_16x16x32_bf16 v[236:239], v[244:247], v[192:195], v[236:239]\n"
      "s_waitcnt lgkmcnt(2)\n"
      "v_mfma_f32_16x16x32_bf16 v[224:227], v[248:251], v[28:31], v[224:227]\n"
      "ds_read_b64_tr_b16 v[244:245], v252 offset:8192\n"
      "v_mfma_f32_16x16x32_bf16 v[228:231], v[248:251], v[84:87], v[228:231]\n"
      "v_mfma_f32_16x16x32_bf16 v[232:235], v[248:251], v[140:143], v[232:235]\n"
      "ds_read_b64_tr_b16 v[246:247], v252 offset:10240\n"
      "v_mfma_f32_16x16x32_bf16 v[236:239], v[248:251], v[196:199], v[236:239]\n"
      "s_waitcnt lgkmcnt(2)\n"
      "v_mfma_f32_16x16x32_bf16 v[224:227], v[240:243], v[32:35], v[224:227]\n"
      "ds_read_b64_tr_b16 v[248:249], v252 offset:12288\n"
      "v_mfma_f32_16x16x32_bf16 v[228:231], v[240:243], v[88:91], v[228:231]\n"
      "v_mfma_f32_16x16x32_bf16 v[232:235], v[240:243], v[144:147], v[232:235]\n"
      "ds_read_b64_tr_b16 v[250:251], v252 offset:14336\n"
      "v_mfma_f32_16x16x32_bf16 v[236:239], v[240:243], v[200:203], v[236:239]\n"
      "s_waitcnt lgkmcnt(2)\n"
      "v_mfma_f32_16x16x32_bf16 v[224:227], v[244:247], v[36:39], v[224:227]\n"
      "ds_read_b64_tr_b16 v[240:241], v252 offset:16384\n"
      "v_mfma_f32_16x16x32_bf16 v[228:231], v[244:247], v[92:95], v[228:231]\n"
      "v_mfma_f32_16x16x32_bf16 v[232:235], v[244:247], v[148:151], v[232:235]\n"
      "ds_read_b64_tr_b16 v[242:243], v252 offset:18432\n"
      "v_mfma_f32_16x16x32_bf16 v[236:239], v[244:247], v[204:207], v[236:239]\n"
      "s_waitcnt lgkmcnt(2)\n"
      "v_mfma_f32_16x16x32_bf16 v[224:227], v[248:251], v[40:43], v[224:227]\n"
      "ds_read_b64_tr_b16 v[244:245], v252 offset:20480\n"
      "v_mfma_f32_16x16x32_bf16 v[228:231], v[248:251], v[96:99], v[228:231]\n"
      "v_mfma_f32_16x16x32_bf16 v[232:235], v[248:251], v[152:155], v[232:235]\n"
      "ds_read_b64_tr_b16 v[246:247], v252 offset:22528\n"
      "v_mfma_f32_16x16x32_bf16 v[236:239], v[248:251], v[208:211], v[236:239]\n"
      "s_waitcnt lgkmcnt(2)\n"
      "v_mfma_f32_16x16x32_bf16 v[224:227], v[240:243], v[44:47], v[224:227]\n"
      "ds_read_b64_tr_b16 v[248:249], v252 offset:24576\n"
      "v_mfma_f32_16x16x32_bf16 v[228:231], v[240:243], v[100:103], v[228:231]\n"
      "v_mfma_f32_16x16x32_bf16 v[232:235], v[240:243], v[156:159], v[232:235]\n"
      "ds_read_b64_tr_b16 v[250:251], v252 offset:26624\n"
      "v_mfma_f32_16x16x32_bf16 v[236:239], v[240:243], v[212:215], v[236:239]\n"
      "s_waitcnt lgkmcnt(2)\n"
      "v_mfma_f32_16x16x32_bf16 v[224:227], v[244:247], v[48:51], v[224:227]\n"
      "ds_read_b64_tr_b16 v[240:241], v255 offset:28672\n"
      "v_mfma_f32_16x16x32_bf16 v[228:231], v[244:247], v[104:107], v[228:231]\n"
      "v_mfma_f32_16x16x32_bf16 v[232:235], v[244:247], v[160:163], v[232:235]\n"
      "ds_read_b64_tr_b16 v[242:243], v255 offset:30720\n"
      "v_mfma_f32_16x16x32_bf16 v[236:239], v[244:247], v[216:219], v[236:239]\n"
      "s_waitcnt lgkmcnt(2)\n"
      "v_mfma_f32_16x16x32_bf16 v[224:227], v[248:251], v[52:55], v[224:227]\n"
      "ds_read_b64_tr_b16 v[244:245], v255 offset:32768\n"
      "v_mfma_f32_16x16x32_bf16 v[228:231], v[248:251], v[108:111], v[228:231]\n"
      "v_mfma_f32_16x16x32_bf16 v[232:235], v[248:251], v[164:167], v[232:235]\n"
      "ds_read_b64_tr_b16 v[246:247], v255 offset:34816\n"
      "v_mfma_f32_16x16x32_bf16 v[236:239], v[248:251], v[220:223], v[236:239]\n"
      "s_nop 2\n"
      "v_cvt_pk_bf16_f32 v224, v224, v225\n"
      "v_cvt_pk_bf16_f32 v225, v226, v227\n"
      "v_cvt_pk_bf16_f32 v228, v228, v229\n"
      "v_cvt_pk_bf16_f32 v229, v230, v231\n"
      "ds_write_addtid_b32 v224 offset:17344\n"
      "ds_write_addtid_b32 v225 offset:17608\n"
      "s_waitcnt lgkmcnt(4)\n"
      "v_mfma_f32_16x16x32_bf16 v[224:227], v[240:243], v[0:3], 0\n"
      "ds_write_addtid_b32 v228 offset:19520\n"
      "ds_write_addtid_b32 v229 offset:19784\n"
      "v_cvt_pk_bf16_f32 v232, v232, v233\n"
      "v_cvt_pk_bf16_f32 v233, v234, v235\n"
      "v_mfma_f32_16x16x32_bf16 v[228:231], v[240:243], v[56:59], 0\n"
      "ds_write_addtid_b32 v232 offset:21696\n"
      "ds_write_addtid_b32 v233 offset:21960\n"
      "v_cvt_pk_bf16_f32 v236, v236, v237\n"
      "v_cvt_pk_bf16_f32 v237, v238, v239\n"
      "v_mfma_f32_16x16x32_bf16 v[232:235], v[240:243], v[112:115], 0\n"
      "ds_write_addtid_b32 v236 offset:23872\n"
      "ds_write_addtid_b32 v237 offset:24136\n"
      "v_mfma_f32_16x16x32_bf16 v[236:239], v[240:243], v[168:171], 0\n"
      "ds_read_b64_tr_b16 v[248:249], v255 offset:36864\n"
      "ds_read_b64_tr_b16 v[250:251], v255 offset:38912\n"
      "s_waitcnt lgkmcnt(10)\n"
      "v_mfma_f32_16x16x32_bf16 v[224:227], v[244:247], v[4:7], v[224:227]\n"
      "ds_read_b64_tr_b16 v[240:241], v255 offset:40960\n"
      "v_mfma_f32_16x16x32_bf16 v[228:231], v[244:247], v[60:63], v[228:231]\n"
      "s_waitcnt lgkmcnt(3)\n"
      "s_barrier\n"
      "v_mfma_f32_16x16x32_bf16 v[232:235], v[244:247], v[116:119], v[232:235]\n"
      "ds_read_b64_tr_b16 v[242:243], v255 offset:43008\n"
      "v_mfma_f32_16x16x32_bf16 v[236:239], v[244:247], v[172:175], v[236:239]\n"
      "s_waitcnt lgkmcnt(2)\n"
      "v_mfma_f32_16x16x32_bf16 v[224:227], v[248:251], v[8:11], v[224:227]\n"
      "ds_read_b64_tr_b16 v[244:245], v255 offset:45056\n"
      "v_mfma_f32_16x16x32_bf16 v[228:231], v[248:251], v[64:67], v[228:231]\n"
      "v_mfma_f32_16x16x32_bf16 v[232:235], v[248:251], v[120:123], v[232:235]\n"
      "ds_read_b64_tr_b16 v[246:247], v255 offset:47104\n"
      "v_mfma_f32_16x16x32_bf16 v[236:239], v[248:251], v[176:179], v[236:239]\n"
      "s_waitcnt lgkmcnt(2)\n"
      "v_mfma_f32_16x16x32_bf16 v[224:227], v[240:243], v[12:15], v[224:227]\n"
      "ds_read_b64_tr_b16 v[248:249], v255 offset:49152\n"
      "v_mfma_f32_16x16x32_bf16 v[228:231], v[240:243], v[68:71], v[228:231]\n"
      "v_mfma_f32_16x16x32_bf16 v[232:235], v[240:243], v[124:127], v[232:235]\n"
      "ds_read_b64_tr_b16 v[250:251], v255 offset:51200\n"
      "v_mfma_f32_16x16x32_bf16 v[236:239], v[240:243], v[180:183], v[236:239]\n"
      "s_waitcnt lgkmcnt(2)\n"
      "v_mfma_f32_16x16x32_bf16 v[224:227], v[244:247], v[16:19], v[224:227]\n"
      "ds_read_b64_tr_b16 v[240:241], v255 offset:53248\n"
      "v_mfma_f32_16x16x32_bf16 v[228:231], v[244:247], v[72:75], v[228:231]\n"
      "v_mfma_f32_16x16x32_bf16 v[232:235], v[244:247], v[128:131], v[232:235]\n"
      "ds_read_b64_tr_b16 v[242:243], v255 offset:55296\n"
      "v_mfma_f32_16x16x32_bf16 v[236:239], v[244:247], v[184:187], v[236:239]\n"
      "s_waitcnt lgkmcnt(2)\n"
      "v_mfma_f32_16x16x32_bf16 v[224:227], v[248:251], v[20:23], v[224:227]\n"
      "ds_read_b64_tr_b16 v[244:245], v253 offset:0\n"
      "v_mfma_f32_16x16x32_bf16 v[228:231], v[248:251], v[76:79], v[228:231]\n"
      "v_mfma_f32_16x16x32_bf16 v[232:235], v[248:251], v[132:135], v[232:235]\n"
      "ds_read_b64_tr_b16 v[246:247], v253 offset:2048\n"
      "v_mfma_f32_16x16x32_bf16 v[236:239], v[248:251], v[188:191], v[236:239]\n"
      "s_waitcnt lgkmcnt(2)\n"
      "v_mfma_f32_16x16x32_bf16 v[224:227], v[240:243], v[24:27], v[224:227]\n"
      "ds_read_b64_tr_b16 v[248:249], v253 offset:4096\n"
      "v_mfma_f32_16x16x32_bf16 v[228:231], v[240:243], v[80:83], v[228:231]\n"
      "v_mfma_f32_16x16x32_bf16 v[232:235], v[240:243], v[136:139], v[232:235]\n"
      "ds_read_b64_tr_b16 v[250:251], v253 offset:6144\n"
      "v_mfma_f32_16x16x32_bf16 v[236:239], v[240:243], v[192:195], v[236:239]\n"
      "s_waitcnt lgkmcnt(2)\n"
      "v_mfma_f32_16x16x32_bf16 v[224:227], v[244:247], v[28:31], v[224:227]\n"
      "ds_read_b64_tr_b16 v[240:241], v253 offset:8192\n"
      "v_mfma_f32_16x16x32_bf16 v[228:231], v[244:247], v[84:87], v[228:231]\n"
      "v_mfma_f32_16x16x32_bf16 v[232:235], v[244:247], v[140:143], v[232:235]\n"
      "ds_read_b64_tr_b16 v[242:243], v253 offset:10240\n"
      "v_mfma_f32_16x16x32_bf16 v[236:239], v[244:247], v[196:199], v[236:239]\n"
      "s_waitcnt lgkmcnt(2)\n"
      "v_mfma_f32_16x16x32_bf16 v[224:227], v[248:251], v[32:35], v[224:227]\n"
      "ds_read_b64_tr_b16 v[244:245], v253 offset:12288\n"
      "v_mfma_f32_16x16x32_bf16 v[228:231], v[248:251], v[88:91], v[228:231]\n"
      "v_mfma_f32_16x16x32_bf16 v[232:235], v[248:251], v[144:147], v[232:235]\n"
      "ds_read_b64_tr_b16 v[246:247], v253 offset:14336\n"
      "v_mfma_f32_16x16x32_bf16 v[236:239], v[248:251], v[200:203], v[236:239]\n"
      "s_waitcnt lgkmcnt(2)\n"
      "v_mfma_f32_16x16x32_bf16 v[224:227], v[240:243], v[36:39], v[224:227]\n"
      "ds_read_b64_tr_b16 v[248:249], v253 offset:16384\n"
      "v_mfma_f32_16x16x32_bf16 v[228:231], v[240:243], v[92:95], v[228:231]\n"
      "v_mfma_f32_16x16x32_bf16 v[232:235], v[240:243], v[148:151], v[232:235]\n"
      "ds_read_b64_tr_b16 v[250:251], v253 offset:18432\n"
      "v_mfma_f32_16x16x32_bf16 v[236:239], v[240:243], v[204:207], v[236:239]\n"
      "s_waitcnt lgkmcnt(2)\n"
      "v_mfma_f32_16x16x32_bf16 v[224:227], v[244:247], v[40:43], v[224:227]\n"
      "ds_read_b64_tr_b16 v[240:241], v253 offset:20480\n"
      "v_mfma_f32_16x16x32_bf16 v[228:231], v[244:247], v[96:99], v[228:231]\n"
      "v_mfma_f32_16x16x32_bf16 v[232:235], v[244:247], v[152:155], v[232:235]\n"
      "ds_read_b64_tr_b16 v[242:243], v253 offset:22528\n"
      "v_mfma_f32_16x16x32_bf16 v[236:239], v[244:247], v[208:211], v[236:239]\n"
      "s_waitcnt lgkmcnt(2)\n"
      "v_mfma_f32_16x16x32_bf16 v[224:227], v[248:251], v[44:47], v[224:227]\n"
      "ds_read_b64_tr_b16 v[244:245], v253 offset:24576\n"
      "v_mfma_f32_16x16x32_bf16 v[228:231], v[248:251], v[100:103], v[228:231]\n"
      "v_mfma_f32_16x16x32_bf16 v[232:235], v[248:251], v[156:159], v[232:235]\n"
      "ds_read_b64_tr_b16 v[246:247], v253 offset:26624\n"
      "v_mfma_f32_16x16x32_bf16 v[236:239], v[248:251], v[212:215], v[236:239]\n"
      "s_waitcnt lgkmcnt(2)\n"
      "v_mfma_f32_16x16x32_bf16 v[224:227], v[240:243], v[48:51], v[224:227]\n"
      "ds_read_b64_tr_b16 v[248:249], v252 offset:28672\n"
      "v_mfma_f32_16x16x32_bf16 v[228:231], v[240:243], v[104:107], v[228:231]\n"
      "v_mfma_f32_16x16x32_bf16 v[232:235], v[240:243], v[160:163], v[232:235]\n"
      "ds_read_b64_tr_b16 v[250:251], v252 offset:30720\n"
      "v_mfma_f32_16x16x32_bf16 v[236:239], v[240:243], v[216:219], v[236:239]\n"
      "s_waitcnt lgkmcnt(2)\n"
      "v_mfma_f32_16x16x32_bf16 v[224:227], v[244:247], v[52:55], v[224:227]\n"
      "ds_read_b64_tr_b16 v[240:241], v252 offset:32768\n"
      "v_mfma_f32_16x16x32_bf16 v[228:231], v[244:247], v[108:111], v[228:231]\n"
      "v_mfma_f32_16x16x32_bf16 v[232:235], v[244:247], v[164:167], v[232:235]\n"
      "ds_read_b64_tr_b16 v[242:243], v252 offset:34816\n"
      "v_mfma_f32_16x16x32_bf16 v[236:239], v[244:247], v[220:223], v[236:239]\n"
      "s_nop 2\n"
      "v_cvt_pk_bf16_f32 v224, v224, v225\n"
      "v_cvt_pk_bf16_f32 v225, v226, v227\n"
      "v_cvt_pk_bf16_f32 v228, v228, v229\n"
      "v_cvt_pk_bf16_f32 v229, v230, v231\n"
      "ds_write_addtid_b32 v224 offset:18400\n"
      "ds_write_addtid_b32 v225 offset:18664\n"
      "s_waitcnt lgkmcnt(4)\n"
      "v_mfma_f32_16x16x32_bf16 v[224:227], v[248:251], v[0:3], 0\n"
      "ds_write_addtid_b32 v228 offset:20576\n"
      "ds_write_addtid_b32 v229 offset:20840\n"
      "v_cvt_pk_bf16_f32 v232, v232, v233\n"
      "v_cvt_pk_bf16_f32 v233, v234, v235\n"
      "v_mfma_f32_16x16x32_bf16 v[228:231], v[248:251], v[56:59], 0\n"
      "ds_write_addtid_b32 v232 offset:22752\n"
      "ds_write_addtid_b32 v233 offset:23016\n"
      "v_cvt_pk_bf16_f32 v236, v236, v237\n"
      "v_cvt_pk_bf16_f32 v237, v238, v239\n"
      "v_mfma_f32_16x16x32_bf16 v[232:235], v[248:251], v[112:115], 0\n"
      "ds_write_addtid_b32 v236 offset:24928\n"
      "ds_write_addtid_b32 v237 offset:25192\n"
      "v_mfma_f32_16x16x32_bf16 v[236:239], v[248:251], v[168:171], 0\n"
      "ds_read_b64_tr_b16 v[244:245], v252 offset:36864\n"
      "ds_read_b64_tr_b16 v[246:247], v252 offset:38912\n"
      "s_waitcnt lgkmcnt(10)\n"
      "v_mfma_f32_16x16x32_bf16 v[224:227], v[240:243], v[4:7], v[224:227]\n"
      "ds_read_b64_tr_b16 v[248:249], v252 offset:40960\n"
      "v_mfma_f32_16x16x32_bf16 v[228:231], v[240:243], v[60:63], v[228:231]\n"
      "s_waitcnt lgkmcnt(3)\n"
      "s_barrier\n"
      "v_mfma_f32_16x16x32_bf16 v[232:235], v[240:243], v[116:119], v[232:235]\n"
      "ds_read_b64_tr_b16 v[250:251], v252 offset:43008\n"
      "v_mfma_f32_16x16x32_bf16 v[236:239], v[240:243], v[172:175], v[236:239]\n"
      "s_waitcnt lgkmcnt(2)\n"
      "v_mfma_f32_16x16x32_bf16 v[224:227], v[244:247], v[8:11], v[224:227]\n"
      "ds_read_b64_tr_b16 v[240:241], v252 offset:45056\n"
      "v_mfma_f32_16x16x32_bf16 v[228:231], v[244:247], v[64:67], v[228:231]\n"
      "v_mfma_f32_16x16x32_bf16 v[232:235], v[244:247], v[120:123], v[232:235]\n"
      "ds_read_b64_tr_b16 v[242:243], v252 offset:47104\n"
      "v_mfma_f32_16x16x32_bf16 v[236:239], v[244:247], v[176:179], v[236:239]\n"
      "s_waitcnt lgkmcnt(2)\n"
      "v_mfma_f32_16x16x32_bf16 v[224:227], v[248:251], v[12:15], v[224:227]\n"
      "ds_read_b64_tr_b16 v[244:245], v252 offset:49152\n"
      "v_mfma_f32_16x16x32_bf16 v[228:231], v[248:251], v[68:71], v[228:231]\n"
      "v_mfma_f32_16x16x32_bf16 v[232:235], v[248:251], v[124:127], v[232:235]\n"
      "ds_read_b64_tr_b16 v[246:247], v252 offset:51200\n"
      "v_mfma_f32_16x16x32_bf16 v[236:239], v[248:251], v[180:183], v[236:239]\n"
      "s_waitcnt lgkmcnt(2)\n"
      "v_mfma_f32_16x16x32_bf16 v[224:227], v[240:243], v[16:19], v[224:227]\n"
      "ds_read_b64_tr_b16 v[248:249], v252 offset:53248\n"
      "v_mfma_f32_16x16x32_bf16 v[228:231], v[240:243], v[72:75], v[228:231]\n"
      "v_mfma_f32_16x16x32_bf16 v[232:235], v[240:243], v[128:131], v[232:235]\n"
      "ds_read_b64_tr_b16 v[250:251], v252 offset:55296\n"
      "v_mfma_f32_16x16x32_bf16 v[236:239], v[240:243], v[184:187], v[236:239]\n"
      "s_waitcnt lgkmcnt(2)\n"
      "v_mfma_f32_16x16x32_bf16 v[224:227], v[244:247], v[20:23], v[224:227]\n"
      "ds_read_b64_tr_b16 v[240:241], v254 offset:0\n"
      "v_mfma_f32_16x16x32_bf16 v[228:231], v[244:247], v[76:79], v[228:231]\n"
      "v_mfma_f32_16x16x32_bf16 v[232:235], v[244:247], v[132:135], v[232:235]\n"
      "ds_read_b64_tr_b16 v[242:243], v254 offset:2048\n"
      "v_mfma_f32_16x16x32_bf16 v[236:239], v[244:247], v[188:191], v[236:239]\n"
      "s_waitcnt lgkmcnt(2)\n"
      "v_mfma_f32_16x16x32_bf16 v[224:227], v[248:251], v[24:27], v[224:227]\n"
      "ds_read_b64_tr_b16 v[244:245], v254 offset:4096\n"
      "v_mfma_f32_16x16x32_bf16 v[228:231], v[248:251], v[80:83], v[228:231]\n"
      "v_mfma_f32_16x16x32_bf16 v[232:235], v[248:251], v[136:139], v[232:235]\n"
      "ds_read_b64_tr_b16 v[246:247], v254 offset:6144\n"
      "v_mfma_f32_16x16x32_bf16 v[236:239], v[248:251], v[192:195], v[236:239]\n"
      "s_waitcnt lgkmcnt(2)\n"
      "v_mfma_f32_16x16x32_bf16 v[224:227], v[240:243], v[28:31], v[224:227]\n"
      "ds_read_b64_tr_b16 v[248:249], v254 offset:8192\n"
      "v_mfma_f32_16x16x32_bf16 v[228:231], v[240:243], v[84:87], v[228:231]\n"
      "v_mfma_f32_16x16x32_bf16 v[232:235], v[240:243], v[140:143], v[232:235]\n"
      "ds_read_b64_tr_b16 v[250:251], v254 offset:10240\n"
      "v_mfma_f32_16x16x32_bf16 v[236:239], v[240:243], v[196:199], v[236:239]\n"
      "s_waitcnt lgkmcnt(2)\n"
      "v_mfma_f32_16x16x32_bf16 v[224:227], v[244:247], v[32:35], v[224:227]\n"
      "ds_read_b64_tr_b16 v[240:241], v254 offset:12288\n"
      "v_mfma_f32_16x16x32_bf16 v[228:231], v[244:247], v[88:91], v[228:231]\n"
      "v_mfma_f32_16x16x32_bf16 v[232:235], v[244:247], v[144:147], v[232:235]\n"
      "ds_read_b64_tr_b16 v[242:243], v254 offset:14336\n"
      "v_mfma_f32_16x16x32_bf16 v[236:239], v[244:247], v[200:203], v[236:239]\n"
      "s_waitcnt lgkmcnt(2)\n"
      "v_mfma_f32_16x16x32_bf16 v[224:227], v[248:251], v[36:39], v[224:227]\n"
      "ds_read_b64_tr_b16 v[244:245], v254 offset:16384\n"
      "v_mfma_f32_16x16x32_bf16 v[228:231], v[248:251], v[92:95], v[228:231]\n"
      "v_mfma_f32_16x16x32_bf16 v[232:235], v[248:251], v[148:151], v[232:235]\n"
      "ds_read_b64_tr_b16 v[246:247], v254 offset:18432\n"
      "v_mfma_f32_16x16x32_bf16 v[236:239], v[248:251], v[204:207], v[236:239]\n"
      "s_waitcnt lgkmcnt(2)\n"
      "v_mfma_f32_16x16x32_bf16 v[224:227], v[240:243], v[40:43], v[224:227]\n"
      "ds_read_b64_tr_b16 v[248:249], v254 offset:20480\n"
      "v_mfma_f32_16x16x32_bf16 v[228:231], v[240:243], v[96:99], v[228:231]\n"
      "v_mfma_f32_16x16x32_bf16 v[232:235], v[240:243], v[152:155], v[232:235]\n"
      "ds_read_b64_tr_b16 v[250:251], v254 offset:22528\n"
      "v_mfma_f32_16x16x32_bf16 v[236:239], v[240:243], v[208:211], v[236:239]\n"
      "s_waitcnt lgkmcnt(2)\n"
      "v_mfma_f32_16x16x32_bf16 v[224:227], v[244:247], v[44:47], v[224:227]\n"
      "ds_read_b64_tr_b16 v[240:241], v254 offset:24576\n"
      "v_mfma_f32_16x16x32_bf16 v[228:231], v[244:247], v[100:103], v[228:231]\n"
      "v_mfma_f32_16x16x32_bf16 v[232:235], v[244:247], v[156:159], v[232:235]\n"
      "ds_read_b64_tr_b16 v[242:243], v254 offset:26624\n"
      "v_mfma_f32_16x16x32_bf16 v[236:239], v[244:247], v[212:215], v[236:239]\n"
      "s_waitcnt lgkmcnt(2)\n"
      "v_mfma_f32_16x16x32_bf16 v[224:227], v[248:251], v[48:51], v[224:227]\n"
      "ds_read_b64_tr_b16 v[244:245], v253 offset:28672\n"
      "v_mfma_f32_16x16x32_bf16 v[228:231], v[248:251], v[104:107], v[228:231]\n"
      "v_mfma_f32_16x16x32_bf16 v[232:235], v[248:251], v[160:163], v[232:235]\n"
      "ds_read_b64_tr_b16 v[246:247], v253 offset:30720\n"
      "v_mfma_f32_16x16x32_bf16 v[236:239], v[248:251], v[216:219], v[236:239]\n"
      "s_waitcnt lgkmcnt(2)\n"
      "v_mfma_f32_16x16x32_bf16 v[224:227], v[240:243], v[52:55], v[224:227]\n"
      "ds_read_b64_tr_b16 v[248:249], v253 offset:32768\n"
      "v_mfma_f32_16x16x32_bf16 v[228:231], v[240:243], v[108:111], v[228:231]\n"
      "v_mfma_f32_16x16x32_bf16 v[232:235], v[240:243], v[164:167], v[232:235]\n"
      "ds_read_b64_tr_b16 v[250:251], v253 offset:34816\n"
      "v_mfma_f32_16x16x32_bf16 v[236:239], v[240:243], v[220:223], v[236:239]\n"
      "s_nop 2\n"
      "v_cvt_pk_bf16_f32 v224, v224, v225\n"
      "v_cvt_pk_bf16_f32 v225, v226, v227\n"
      "v_cvt_pk_bf16_f32 v228, v228, v229\n"
      "v_cvt_pk_bf16_f32 v229, v230, v231\n"
      "ds_write_addtid_b32 v224 offset:34752\n"
      "ds_write_addtid_b32 v225 offset:35016\n"
      "s_waitcnt lgkmcnt(4)\n"
      "v_mfma_f32_16x16x32_bf16 v[224:227], v[244:247], v[0:3], 0\n"
      "ds_write_addtid_b32 v228 offset:36928\n"
      "ds_write_addtid_b32 v229 offset:37192\n"
      "v_cvt_pk_bf16_f32 v232, v232, v233\n"
      "v_cvt_pk_bf16_f32 v233, v234, v235\n"
      "v_mfma_f32_16x16x32_bf16 v[228:231], v[244:247], v[56:59], 0\n"
      "ds_write_addtid_b32 v232 offset:39104\n"
      "ds_write_addtid_b32 v233 offset:39368\n"
      "v_cvt_pk_bf16_f32 v236, v236, v237\n"
      "v_cvt_pk_bf16_f32 v237, v238, v239\n"
      "v_mfma_f32_16x16x32_bf16 v[232:235], v[244:247], v[112:115], 0\n"
      "ds_write_addtid_b32 v236 offset:41280\n"
      "ds_write_addtid_b32 v237 offset:41544\n"
      "v_mfma_f32_16x16x32_bf16 v[236:239], v[244:247], v[168:171], 0\n"
      "ds_read_b64_tr_b16 v[240:241], v253 offset:36864\n"
      "ds_read_b64_tr_b16 v[242:243], v253 offset:38912\n"
      "s_waitcnt lgkmcnt(10)\n"
      "v_mfma_f32_16x16x32_bf16 v[224:227], v[248:251], v[4:7], v[224:227]\n"
      "ds_read_b64_tr_b16 v[244:245], v253 offset:40960\n"
      "v_mfma_f32_16x16x32_bf16 v[228:231], v[248:251], v[60:63], v[228:231]\n"
      "s_waitcnt lgkmcnt(3)\n"
      "s_barrier\n"
      "v_mfma_f32_16x16x32_bf16 v[232:235], v[248:251], v[116:119], v[232:235]\n"
      "ds_read_b64_tr_b16 v[246:247], v253 offset:43008\n"
      "v_mfma_f32_16x16x32_bf16 v[236:239], v[248:251], v[172:175], v[236:239]\n"
      "s_waitcnt lgkmcnt(2)\n"
      "v_mfma_f32_16x16x32_bf16 v[224:227], v[240:243], v[8:11], v[224:227]\n"
      "ds_read_b64_tr_b16 v[248:249], v253 offset:45056\n"
      "v_mfma_f32_16x16x32_bf16 v[228:231], v[240:243], v[64:67], v[228:231]\n"
      "v_mfma_f32_16x16x32_bf16 v[232:235], v[240:243], v[120:123], v[232:235]\n"
      "ds_read_b64_tr_b16 v[250:251], v253 offset:47104\n"
      "v_mfma_f32_16x16x32_bf16 v[236:239], v[240:243], v[176:179], v[236:239]\n"
      "s_waitcnt lgkmcnt(2)\n"
      "v_mfma_f32_16x16x32_bf16 v[224:227], v[244:247], v[12:15], v[224:227]\n"
      "ds_read_b64_tr_b16 v[240:241], v253 offset:49152\n"
      "v_mfma_f32_16x16x32_bf16 v[228:231], v[244:247], v[68:71], v[228:231]\n"
      "v_mfma_f32_16x16x32_bf16 v[232:235], v[244:247], v[124:127], v[232:235]\n"
      "ds_read_b64_tr_b16 v[242:243], v253 offset:51200\n"
      "v_mfma_f32_16x16x32_bf16 v[236:239], v[244:247], v[180:183], v[236:239]\n"
      "s_waitcnt lgkmcnt(2)\n"
      "v_mfma_f32_16x16x32_bf16 v[224:227], v[248:251], v[16:19], v[224:227]\n"
      "ds_read_b64_tr_b16 v[244:245], v253 offset:53248\n"
      "v_mfma_f32_16x16x32_bf16 v[228:231], v[248:251], v[72:75], v[228:231]\n"
      "v_mfma_f32_16x16x32_bf16 v[232:235], v[248:251], v[128:131], v[232:235]\n"
      "ds_read_b64_tr_b16 v[246:247], v253 offset:55296\n"
      "v_mfma_f32_16x16x32_bf16 v[236:239], v[248:251], v[184:187], v[236:239]\n"
      "s_waitcnt lgkmcnt(2)\n"
      "v_mfma_f32_16x16x32_bf16 v[224:227], v[240:243], v[20:23], v[224:227]\n"
      "ds_read_b64_tr_b16 v[248:249], v255 offset:0\n"
      "v_mfma_f32_16x16x32_bf16 v[228:231], v[240:243], v[76:79], v[228:231]\n"
      "v_mfma_f32_16x16x32_bf16 v[232:235], v[240:243], v[132:135], v[232:235]\n"
      "ds_read_b64_tr_b16 v[250:251], v255 offset:2048\n"
      "v_mfma_f32_16x16x32_bf16 v[236:239], v[240:243], v[188:191], v[236:239]\n"
      "s_waitcnt lgkmcnt(2)\n"
      "v_mfma_f32_16x16x32_bf16 v[224:227], v[244:247], v[24:27], v[224:227]\n"
      "ds_read_b64_tr_b16 v[240:241], v255 offset:4096\n"
      "v_mfma_f32_16x16x32_bf16 v[228:231], v[244:247], v[80:83], v[228:231]\n"
      "v_mfma_f32_16x16x32_bf16 v[232:235], v[244:247], v[136:139], v[232:235]\n"
      "ds_read_b64_tr_b16 v[242:243], v255 offset:6144\n"
      "v_mfma_f32_16x16x32_bf16 v[236:239], v[244:247], v[192:195], v[236:239]\n"
      "s_waitcnt lgkmcnt(2)\n"
      "v_mfma_f32_16x16x32_bf16 v[224:227], v[248:251], v[28:31], v[224:227]\n"
      "ds_read_b64_tr_b16 v[244:245], v255 offset:8192\n"
      "v_mfma_f32_16x16x32_bf16 v[228:231], v[248:251], v[84:87], v[228:231]\n"
      "v_mfma_f32_16x16x32_bf16 v[232:235], v[248:251], v[140:143], v[232:235]\n"
      "ds_read_b64_tr_b16 v[246:247], v255 offset:10240\n"
      "v_mfma_f32_16x16x32_bf16 v[236:239], v[248:251], v[196:199], v[236:239]\n"
      "s_waitcnt lgkmcnt(2)\n"
      "v_mfma_f32_16x16x32_bf16 v[224:227], v[240:243], v[32:35], v[224:227]\n"
      "ds_read_b64_tr_b16 v[248:249], v255 offset:12288\n"
      "v_mfma_f32_16x16x32_bf16 v[228:231], v[240:243], v[88:91], v[228:231]\n"
      "v_mfma_f32_16x16x32_bf16 v[232:235], v[240:243], v[144:147], v[232:235]\n"
      "ds_read_b64_tr_b16 v[250:251], v255 offset:14336\n"
      "v_mfma_f32_16x16x32_bf16 v[236:239], v[240:243], v[200:203], v[236:239]\n"
      "s_waitcnt lgkmcnt(2)\n"
      "v_mfma_f32_16x16x32_bf16 v[224:227], v[244:247], v[36:39], v[224:227]\n"
      "ds_read_b64_tr_b16 v[240:241], v255 offset:16384\n"
      "v_mfma_f32_16x16x32_bf16 v[228:231], v[244:247], v[92:95], v[228:231]\n"
      "v_mfma_f32_16x16x32_bf16 v[232:235], v[244:247], v[148:151], v[232:235]\n"
      "ds_read_b64_tr_b16 v[242:243], v255 offset:18432\n"
      "v_mfma_f32_16x16x32_bf16 v[236:239], v[244:247], v[204:207], v[236:239]\n"
      "s_waitcnt lgkmcnt(2)\n"
      "v_mfma_f32_16x16x32_bf16 v[224:227], v[248:251], v[40:43], v[224:227]\n"
      "ds_read_b64_tr_b16 v[244:245], v255 offset:20480\n"
      "v_mfma_f32_16x16x32_bf16 v[228:231], v[248:251], v[96:99], v[228:231]\n"
      "v_mfma_f32_16x16x32_bf16 v[232:235], v[248:251], v[152:155], v[232:235]\n"
      "ds_read_b64_tr_b16 v[246:247], v255 offset:22528\n"
      "v_mfma_f32_16x16x32_bf16 v[236:239], v[248:251], v[208:211], v[236:239]\n"
      "s_waitcnt lgkmcnt(2)\n"
      "v_mfma_f32_16x16x32_bf16 v[224:227], v[240:243], v[44:47], v[224:227]\n"
      "ds_read_b64_tr_b16 v[248:249], v255 offset:24576\n"
      "v_mfma_f32_16x16x32_bf16 v[228:231], v[240:243], v[100:103], v[228:231]\n"
      "v_mfma_f32_16x16x32_bf16 v[232:235], v[240:243], v[156:159], v[232:235]\n"
      "ds_read_b64_tr_b16 v[250:251], v255 offset:26624\n"
      "v_mfma_f32_16x16x32_bf16 v[236:239], v[240:243], v[212:215], v[236:239]\n"
      "s_waitcnt lgkmcnt(2)\n"
      "v_mfma_f32_16x16x32_bf16 v[224:227], v[244:247], v[48:51], v[224:227]\n"
      "ds_read_b64_tr_b16 v[240:241], v254 offset:28672\n"
      "v_mfma_f32_16x16x32_bf16 v[228:231], v[244:247], v[104:107], v[228:231]\n"
      "v_mfma_f32_16x16x32_bf16 v[232:235], v[244:247], v[160:163], v[232:235]\n"
      "ds_read_b64_tr_b16 v[242:243], v254 offset:30720\n"
      "v_mfma_f32_16x16x32_bf16 v[236:239], v[244:247], v[216:219], v[236:239]\n"
      "s_waitcnt lgkmcnt(2)\n"
      "v_mfma_f32_16x16x32_bf16 v[224:227], v[248:251], v[52:55], v[224:227]\n"
      "ds_read_b64_tr_b16 v[244:245], v254 offset:32768\n"
      "v_mfma_f32_16x16x32_bf16 v[228:231], v[248:251], v[108:111], v[228:231]\n"
      "v_mfma_f32_16x16x32_bf16 v[232:235], v[248:251], v[164:167], v[232:235]\n"
      "ds_read_b64_tr_b16 v[246:247], v254 offset:34816\n"
      "v_mfma_f32_16x16x32_bf16 v[236:239], v[248:251], v[220:223], v[236:239]\n"
      "s_nop 2\n"
      "v_cvt_pk_bf16_f32 v224, v224, v225\n"
      "v_cvt_pk_bf16_f32 v225, v226, v227\n"
      "v_cvt_pk_bf16_f32 v228, v228, v229\n"
      "v_cvt_pk_bf16_f32 v229, v230, v231\n"
      "ds_write_addtid_b32 v224 offset:35808\n"
      "ds_write_addtid_b32 v225 offset:36072\n"
      "s_waitcnt lgkmcnt(4)\n"
      "v_mfma_f32_16x16x32_bf16 v[224:227], v[240:243], v[0:3], 0\n"
      "ds_write_addtid_b32 v228 offset:37984\n"
      "ds_write_addtid_b32 v229 offset:38248\n"
      "v_cvt_pk_bf16_f32 v232, v232, v233\n"
      "v_cvt_pk_bf16_f32 v233, v234, v235\n"
      "v_mfma_f32_16x16x32_bf16 v[228:231], v[240:243], v[56:59], 0\n"
      "ds_write_addtid_b32 v232 offset:40160\n"
      "ds_write_addtid_b32 v233 offset:40424\n"
      "v_cvt_pk_bf16_f32 v236, v236, v237\n"
      "v_cvt_pk_bf16_f32 v237, v238, v239\n"
      "v_mfma_f32_16x16x32_bf16 v[232:235], v[240:243], v[112:115], 0\n"
      "ds_write_addtid_b32 v236 offset:42336\n"
      "ds_write_addtid_b32 v237 offset:42600\n"
      "v_mfma_f32_16x16x32_bf16 v[236:239], v[240:243], v[168:171], 0\n"
      "ds_read_b64_tr_b16 v[248:249], v254 offset:36864\n"
      "ds_read_b64_tr_b16 v[250:251], v254 offset:38912\n"
      "s_waitcnt lgkmcnt(10)\n"
      "v_mfma_f32_16x16x32_bf16 v[224:227], v[244:247], v[4:7], v[224:227]\n"
      "ds_read_b64_tr_b16 v[240:241], v254 offset:40960\n"
      "v_mfma_f32_16x16x32_bf16 v[228:231], v[244:247], v[60:63], v[228:231]\n"
      "s_waitcnt lgkmcnt(3)\n"
      "s_barrier\n"
      "v_mfma_f32_16x16x32_bf16 v[232:235], v[244:247], v[116:119], v[232:235]\n"
      "ds_read_b64_tr_b16 v[242:243], v254 offset:43008\n"
      "v_mfma_f32_16x16x32_bf16 v[236:239], v[244:247], v[172:175], v[236:239]\n"
      "s_waitcnt lgkmcnt(2)\n"
      "v_mfma_f32_16x16x32_bf16 v[224:227], v[248:251], v[8:11], v[224:227]\n"
      "ds_read_b64_tr_b16 v[244:245], v254 offset:45056\n"
      "v_mfma_f32_16x16x32_bf16 v[228:231], v[248:251], v[64:67], v[228:231]\n"
      "v_mfma_f32_16x16x32_bf16 v[232:235], v[248:251], v[120:123], v[232:235]\n"
      "ds_read_b64_tr_b16 v[246:247], v254 offset:47104\n"
      "v_mfma_f32_16x16x32_bf16 v[236:239], v[248:251], v[176:179], v[236:239]\n"
      "s_waitcnt lgkmcnt(2)\n"
      "v_mfma_f32_16x16x32_bf16 v[224:227], v[240:243], v[12:15], v[224:227]\n"
      "ds_read_b64_tr_b16 v[248:249], v254 offset:49152\n"
      "v_mfma_f32_16x16x32_bf16 v[228:231], v[240:243], v[68:71], v[228:231]\n"
      "v_mfma_f32_16x16x32_bf16 v[232:235], v[240:243], v[124:127], v[232:235]\n"
      "ds_read_b64_tr_b16 v[250:251], v254 offset:51200\n"
      "v_mfma_f32_16x16x32_bf16 v[236:239], v[240:243], v[180:183], v[236:239]\n"
      "s_waitcnt lgkmcnt(2)\n"
      "v_mfma_f32_16x16x32_bf16 v[224:227], v[244:247], v[16:19], v[224:227]\n"
      "ds_read_b64_tr_b16 v[240:241], v254 offset:53248\n"
      "v_mfma_f32_16x16x32_bf16 v[228:231], v[244:247], v[72:75], v[228:231]\n"
      "v_mfma_f32_16x16x32_bf16 v[232:235], v[244:247], v[128:131], v[232:235]\n"
      "ds_read_b64_tr_b16 v[242:243], v254 offset:55296\n"
      "v_mfma_f32_16x16x32_bf16 v[236:239], v[244:247], v[184:187], v[236:239]\n"
      "s_waitcnt lgkmcnt(2)\n"
      "v_mfma_f32_16x16x32_bf16 v[224:227], v[248:251], v[20:23], v[224:227]\n"
      "ds_read_b64_tr_b16 v[244:245], v252 offset:0\n"
      "v_mfma_f32_16x16x32_bf16 v[228:231], v[248:251], v[76:79], v[228:231]\n"
      "v_mfma_f32_16x16x32_bf16 v[232:235], v[248:251], v[132:135], v[232:235]\n"
      "ds_read_b64_tr_b16 v[246:247], v252 offset:2048\n"
      "v_mfma_f32_16x16x32_bf16 v[236:239], v[248:251], v[188:191], v[236:239]\n"
      "s_waitcnt lgkmcnt(2)\n"
      "v_mfma_f32_16x16x32_bf16 v[224:227], v[240:243], v[24:27], v[224:227]\n"
      "ds_read_b64_tr_b16 v[248:249], v252 offset:4096\n"
      "v_mfma_f32_16x16x32_bf16 v[228:231], v[240:243], v[80:83], v[228:231]\n"
      "v_mfma_f32_16x16x32_bf16 v[232:235], v[240:243], v[136:139], v[232:235]\n"
      "ds_read_b64_tr_b16 v[250:251], v252 offset:6144\n"
      "v_mfma_f32_16x16x32_bf16 v[236:239], v[240:243], v[192:195], v[236:239]\n"
      "s_waitcnt lgkmcnt(2)\n"
      "v_mfma_f32_16x16x32_bf16 v[224:227], v[244:247], v[28:31], v[224:227]\n"
      "ds_read_b64_tr_b16 v[240:241], v252 offset:8192\n"
      "v_mfma_f32_16x16x32_bf16 v[228:231], v[244:247], v[84:87], v[228:231]\n"
      "v_mfma_f32_16x16x32_bf16 v[232:235], v[244:247], v[140:143], v[232:235]\n"
      "ds_read_b64_tr_b16 v[242:243], v252 offset:10240\n"
      "v_mfma_f32_16x16x32_bf16 v[236:239], v[244:247], v[196:199], v[236:239]\n"
      "s_waitcnt lgkmcnt(2)\n"
      "v_mfma_f32_16x16x32_bf16 v[224:227], v[248:251], v[32:35], v[224:227]\n"
      "ds_read_b64_tr_b16 v[244:245], v252 offset:12288\n"
      "v_mfma_f32_16x16x32_bf16 v[228:231], v[248:251], v[88:91], v[228:231]\n"
      "v_mfma_f32_16x16x32_bf16 v[232:235], v[248:251], v[144:147], v[232:235]\n"
      "ds_read_b64_tr_b16 v[246:247], v252 offset:14336\n"
      "v_mfma_f32_16x16x32_bf16 v[236:239], v[248:251], v[200:203], v[236:239]\n"
      "s_waitcnt lgkmcnt(2)\n"
      "v_mfma_f32_16x16x32_bf16 v[224:227], v[240:243], v[36:39], v[224:227]\n"
      "ds_read_b64_tr_b16 v[248:249], v252 offset:16384\n"
      "v_mfma_f32_16x16x32_bf16 v[228:231], v[240:243], v[92:95], v[228:231]\n"
      "v_mfma_f32_16x16x32_bf16 v[232:235], v[240:243], v[148:151], v[232:235]\n"
      "ds_read_b64_tr_b16 v[250:251], v252 offset:18432\n"
      "v_mfma_f32_16x16x32_bf16 v[236:239], v[240:243], v[204:207], v[236:239]\n"
      "s_waitcnt lgkmcnt(2)\n"
      "v_mfma_f32_16x16x32_bf16 v[224:227], v[244:247], v[40:43], v[224:227]\n"
      "ds_read_b64_tr_b16 v[240:241], v252 offset:20480\n"
      "v_mfma_f32_16x16x32_bf16 v[228:231], v[244:247], v[96:99], v[228:231]\n"
      "v_mfma_f32_16x16x32_bf16 v[232:235], v[244:247], v[152:155], v[232:235]\n"
      "ds_read_b64_tr_b16 v[242:243], v252 offset:22528\n"
      "v_mfma_f32_16x16x32_bf16 v[236:239], v[244:247], v[208:211], v[236:239]\n"
      "s_waitcnt lgkmcnt(2)\n"
      "v_mfma_f32_16x16x32_bf16 v[224:227], v[248:251], v[44:47], v[224:227]\n"
      "ds_read_b64_tr_b16 v[244:245], v252 offset:24576\n"
      "v_mfma_f32_16x16x32_bf16 v[228:231], v[248:251], v[100:103], v[228:231]\n"
      "v_mfma_f32_16x16x32_bf16 v[232:235], v[248:251], v[156:159], v[232:235]\n"
      "ds_read_b64_tr_b16 v[246:247], v252 offset:26624\n"
      "v_mfma_f32_16x16x32_bf16 v[236:239], v[248:251], v[212:215], v[236:239]\n"
      "s_waitcnt lgkmcnt(2)\n"
      "v_mfma_f32_16x16x32_bf16 v[224:227], v[240:243], v[48:51], v[224:227]\n"
      "ds_read_b64_tr_b16 v[248:249], v255 offset:28672\n"
      "v_mfma_f32_16x16x32_bf16 v[228:231], v[240:243], v[104:107], v[228:231]\n"
      "v_mfma_f32_16x16x32_bf16 v[232:235], v[240:243], v[160:163], v[232:235]\n"
      "ds_read_b64_tr_b16 v[250:251], v255 offset:30720\n"
      "v_mfma_f32_16x16x32_bf16 v[236:239], v[240:243], v[216:219], v[236:239]\n"
      "s_waitcnt lgkmcnt(2)\n"
      "v_mfma_f32_16x16x32_bf16 v[224:227], v[244:247], v[52:55], v[224:227]\n"
      "ds_read_b64_tr_b16 v[240:241], v255 offset:32768\n"
      "v_mfma_f32_16x16x32_bf16 v[228:231], v[244:247], v[108:111], v[228:231]\n"
      "v_mfma_f32_16x16x32_bf16 v[232:235], v[244:247], v[164:167], v[232:235]\n"
      "ds_read_b64_tr_b16 v[242:243], v255 offset:34816\n"
      "v_mfma_f32_16x16x32_bf16 v[236:239], v[244:247], v[220:223], v[236:239]\n"
      "s_nop 2\n"
      "v_cvt_pk_bf16_f32 v224, v224, v225\n"
      "v_cvt_pk_bf16_f32 v225, v226, v227\n"
      "v_cvt_pk_bf16_f32 v228, v228, v229\n"
      "v_cvt_pk_bf16_f32 v229, v230, v231\n"
      "ds_write_addtid_b32 v224 offset:17344\n"
      "ds_write_addtid_b32 v225 offset:17608\n"
      "s_waitcnt lgkmcnt(4)\n"
      "v_mfma_f32_16x16x32_bf16 v[224:227], v[248:251], v[0:3], 0\n"
      "ds_write_addtid_b32 v228 offset:19520\n"
      "ds_write_addtid_b32 v229 offset:19784\n"
      "v_cvt_pk_bf16_f32 v232, v232, v233\n"
      "v_cvt_pk_bf16_f32 v233, v234, v235\n"
      "v_mfma_f32_16x16x32_bf16 v[228:231], v[248:251], v[56:59], 0\n"
      "ds_write_addtid_b32 v232 offset:21696\n"
      "ds_write_addtid_b32 v233 offset:21960\n"
      "v_cvt_pk_bf16_f32 v236, v236, v237\n"
      "v_cvt_pk_bf16_f32 v237, v238, v239\n"
      "v_mfma_f32_16x16x32_bf16 v[232:235], v[248:251], v[112:115], 0\n"
      "ds_write_addtid_b32 v236 offset:23872\n"
      "ds_write_addtid_b32 v237 offset:24136\n"
      "v_mfma_f32_16x16x32_bf16 v[236:239], v[248:251], v[168:171], 0\n"
      "ds_read_b64_tr_b16 v[244:245], v255 offset:36864\n"
      "ds_read_b64_tr_b16 v[246:247], v255 offset:38912\n"
      "s_waitcnt lgkmcnt(10)\n"
      "v_mfma_f32_16x16x32_bf16 v[224:227], v[240:243], v[4:7], v[224:227]\n"
      "ds_read_b64_tr_b16 v[248:249], v255 offset:40960\n"
      "v_mfma_f32_16x16x32_bf16 v[228:231], v[240:243], v[60:63], v[228:231]\n"
      "s_waitcnt lgkmcnt(3)\n"
      "s_barrier\n"
      "v_mfma_f32_16x16x32_bf16 v[232:235], v[240:243], v[116:119], v[232:235]\n"
      "ds_read_b64_tr_b16 v[250:251], v255 offset:43008\n"
      "v_mfma_f32_16x16x32_bf16 v[236:239], v[240:243], v[172:175], v[236:239]\n"
      "s_waitcnt lgkmcnt(2)\n"
      "v_mfma_f32_16x16x32_bf16 v[224:227], v[244:247], v[8:11], v[224:227]\n"
      "ds_read_b64_tr_b16 v[240:241], v255 offset:45056\n"
      "v_mfma_f32_16x16x32_bf16 v[228:231], v[244:247], v[64:67], v[228:231]\n"
      "v_mfma_f32_16x16x32_bf16 v[232:235], v[244:247], v[120:123], v[232:235]\n"
      "ds_read_b64_tr_b16 v[242:243], v255 offset:47104\n"
      "v_mfma_f32_16x16x32_bf16 v[236:239], v[244:247], v[176:179], v[236:239]\n"
      "s_waitcnt lgkmcnt(2)\n"
      "v_mfma_f32_16x16x32_bf16 v[224:227], v[248:251], v[12:15], v[224:227]\n"
      "ds_read_b64_tr_b16 v[244:245], v255 offset:49152\n"
      "v_mfma_f32_16x16x32_bf16 v[228:231], v[248:251], v[68:71], v[228:231]\n"
      "v_mfma_f32_16x16x32_bf16 v[232:235], v[248:251], v[124:127], v[232:235]\n"
      "ds_read_b64_tr_b16 v[246:247], v255 offset:51200\n"
      "v_mfma_f32_16x16x32_bf16 v[236:239], v[248:251], v[180:183], v[236:239]\n"
      "s_waitcnt lgkmcnt(2)\n"
      "v_mfma_f32_16x16x32_bf16 v[224:227], v[240:243], v[16:19], v[224:227]\n"
      "ds_read_b64_tr_b16 v[248:249], v255 offset:53248\n"
      "v_mfma_f32_16x16x32_bf16 v[228:231], v[240:243], v[72:75], v[228:231]\n"
      "v_mfma_f32_16x16x32_bf16 v[232:235], v[240:243], v[128:131], v[232:235]\n"
      "ds_read_b64_tr_b16 v[250:251], v255 offset:55296\n"
      "v_mfma_f32_16x16x32_bf16 v[236:239], v[240:243], v[184:187], v[236:239]\n"
      "s_waitcnt lgkmcnt(2)\n"
      "v_mfma_f32_16x16x32_bf16 v[224:227], v[244:247], v[20:23], v[224:227]\n"
      "ds_read_b64_tr_b16 v[240:241], v253 offset:0\n"
      "v_mfma_f32_16x16x32_bf16 v[228:231], v[244:247], v[76:79], v[228:231]\n"
      "v_mfma_f32_16x16x32_bf16 v[232:235], v[244:247], v[132:135], v[232:235]\n"
      "ds_read_b64_tr_b16 v[242:243], v253 offset:2048\n"
      "v_mfma_f32_16x16x32_bf16 v[236:239], v[244:247], v[188:191], v[236:239]\n"
      "s_waitcnt lgkmcnt(2)\n"
      "v_mfma_f32_16x16x32_bf16 v[224:227], v[248:251], v[24:27], v[224:227]\n"
      "ds_read_b64_tr_b16 v[244:245], v253 offset:4096\n"
      "v_mfma_f32_16x16x32_bf16 v[228:231], v[248:251], v[80:83], v[228:231]\n"
      "v_mfma_f32_16x16x32_bf16 v[232:235], v[248:251], v[136:139], v[232:235]\n"
      "ds_read_b64_tr_b16 v[246:247], v253 offset:6144\n"
      "v_mfma_f32_16x16x32_bf16 v[236:239], v[248:251], v[192:195], v[236:239]\n"
      "s_waitcnt lgkmcnt(2)\n"
      "v_mfma_f32_16x16x32_bf16 v[224:227], v[240:243], v[28:31], v[224:227]\n"
      "ds_read_b64_tr_b16 v[248:249], v253 offset:8192\n"
      "v_mfma_f32_16x16x32_bf16 v[228:231], v[240:243], v[84:87], v[228:231]\n"
      "v_mfma_f32_16x16x32_bf16 v[232:235], v[240:243], v[140:143], v[232:235]\n"
      "ds_read_b64_tr_b16 v[250:251], v253 offset:10240\n"
      "v_mfma_f32_16x16x32_bf16 v[236:239], v[240:243], v[196:199], v[236:239]\n"
      "s_waitcnt lgkmcnt(2)\n"
      "v_mfma_f32_16x16x32_bf16 v[224:227], v[244:247], v[32:35], v[224:227]\n"
      "ds_read_b64_tr_b16 v[240:241], v253 offset:12288\n"
      "v_mfma_f32_16x16x32_bf16 v[228:231], v[244:247], v[88:91], v[228:231]\n"
      "v_mfma_f32_16x16x32_bf16 v[232:235], v[244:247], v[144:147], v[232:235]\n"
      "ds_read_b64_tr_b16 v[242:243], v253 offset:14336\n"
      "v_mfma_f32_16x16x32_bf16 v[236:239], v[244:247], v[200:203], v[236:239]\n"
      "s_waitcnt lgkmcnt(2)\n"
      "v_mfma_f32_16x16x32_bf16 v[224:227], v[248:251], v[36:39], v[224:227]\n"
      "ds_read_b64_tr_b16 v[244:245], v253 offset:16384\n"
      "v_mfma_f32_16x16x32_bf16 v[228:231], v[248:251], v[92:95], v[228:231]\n"
      "v_mfma_f32_16x16x32_bf16 v[232:235], v[248:251], v[148:151], v[232:235]\n"
      "ds_read_b64_tr_b16 v[246:247], v253 offset:18432\n"
      "v_mfma_f32_16x16x32_bf16 v[236:239], v[248:251], v[204:207], v[236:239]\n"
      "s_waitcnt lgkmcnt(2)\n"
      "v_mfma_f32_16x16x32_bf16 v[224:227], v[240:243], v[40:43], v[224:227]\n"
      "ds_read_b64_tr_b16 v[248:249], v253 offset:20480\n"
      "v_mfma_f32_16x16x32_bf16 v[228:231], v[240:243], v[96:99], v[228:231]\n"
      "v_mfma_f32_16x16x32_bf16 v[232:235], v[240:243], v[152:155], v[232:235]\n"
      "ds_read_b64_tr_b16 v[250:251], v253 offset:22528\n"
      "v_mfma_f32_16x16x32_bf16 v[236:239], v[240:243], v[208:211], v[236:239]\n"
      "s_waitcnt lgkmcnt(2)\n"
      "v_mfma_f32_16x16x32_bf16 v[224:227], v[244:247], v[44:47], v[224:227]\n"
      "ds_read_b64_tr_b16 v[240:241], v253 offset:24576\n"
      "v_mfma_f32_16x16x32_bf16 v[228:231], v[244:247], v[100:103], v[228:231]\n"
      "v_mfma_f32_16x16x32_bf16 v[232:235], v[244:247], v[156:159], v[232:235]\n"
      "ds_read_b64_tr_b16 v[242:243], v253 offset:26624\n"
      "v_mfma_f32_16x16x32_bf16 v[236:239], v[244:247], v[212:215], v[236:239]\n"
      "s_waitcnt lgkmcnt(2)\n"
      "v_mfma_f32_16x16x32_bf16 v[224:227], v[248:251], v[48:51], v[224:227]\n"
      "v_mfma_f32_16x16x32_bf16 v[228:231], v[248:251], v[104:107], v[228:231]\n"
      "v_mfma_f32_16x16x32_bf16 v[232:235], v[248:251], v[160:163], v[232:235]\n"
      "v_mfma_f32_16x16x32_bf16 v[236:239], v[248:251], v[216:219], v[236:239]\n"
      "s_waitcnt lgkmcnt(0)\n"
      "v_mfma_f32_16x16x32_bf16 v[224:227], v[240:243], v[52:55], v[224:227]\n"
      "v_mfma_f32_16x16x32_bf16 v[228:231], v[240:243], v[108:111], v[228:231]\n"
      "v_mfma_f32_16x16x32_bf16 v[232:235], v[240:243], v[164:167], v[232:235]\n"
      "v_mfma_f32_16x16x32_bf16 v[236:239], v[240:243], v[220:223], v[236:239]\n"
      "s_nop 4\n"
      "v_cvt_pk_bf16_f32 v224, v224, v225\n"
      "v_cvt_pk_bf16_f32 v225, v226, v227\n"
      "v_cvt_pk_bf16_f32 v228, v228, v229\n"
      "v_cvt_pk_bf16_f32 v229, v230, v231\n"
      "ds_write_addtid_b32 v224 offset:18400\n"
      "ds_write_addtid_b32 v225 offset:18664\n"
      "v_cvt_pk_bf16_f32 v232, v232, v233\n"
      "v_cvt_pk_bf16_f32 v233, v234, v235\n"
      "ds_write_addtid_b32 v228 offset:20576\n"
      "ds_write_addtid_b32 v229 offset:20840\n"
      "v_cvt_pk_bf16_f32 v236, v236, v237\n"
      "v_cvt_pk_bf16_f32 v237, v238, v239\n"
      "ds_write_addtid_b32 v232 offset:22752\n"
      "ds_write_addtid_b32 v233 offset:23016\n"
      "ds_write_addtid_b32 v236 offset:24928\n"
      "ds_write_addtid_b32 v237 offset:25192\n"
      "v_mbcnt_lo_u32_b32 v0, -1, 0\n"
      "v_mbcnt_hi_u32_b32 v0, -1, v0\n"
      "s_mov_b64 s[48:49], %[op]\n"
      "s_and_b32 s49, s49, 0xffff\n"
      "s_mov_b32 s50, 114688\n"
      "s_mov_b32 s51, 0x20000\n"
      "s_mul_i32 s71, %[w], 4352\n"
      "s_add_u32 s71, s71, 59520\n"
      "v_bfe_u32 v1, v0, 1, 2\n"
      "v_mul_u32_u24 v2, 528, v1\n"
      "v_and_b32 v1, 1, v0\n"
      "v_lshl_add_u32 v2, v1, 7, v2\n"
      "v_lshrrev_b32 v1, 3, v0\n"
      "v_lshl_add_u32 v2, v1, 2, v2\n"
      "v_add_u32 v2, s71, v2\n"
      "v_lshrrev_b32 v1, 5, v0\n"
      "v_bfe_u32 v3, v0, 3, 2\n"
      "v_lshl_add_u32 v3, v1, 3, v3\n"
      "v_mul_u32_u24 v3, 896, v3\n"
      "v_and_b32 v1, 7, v0\n"
      "v_lshl_add_u32 v3, v1, 4, v3\n"
      "s_mul_i32 s71, %[w], 28672\n"
      "v_add_u32 v3, s71, v3\n"
      "s_waitcnt lgkmcnt(0)\n"
      "s_barrier\n"
      "ds_read2_b32 v[8:9], v2 offset0:0 offset1:66\n"
      "ds_read2_b32 v[10:11], v2 offset0:16 offset1:82\n"
      "ds_read2_b32 v[12:13], v2 offset0:8 offset1:74\n"
      "ds_read2_b32 v[14:15], v2 offset0:24 offset1:90\n"
      "s_movk_i32 s68, 4352\n"
      "s_waitcnt lgkmcnt(2)\n"
      "buffer_store_dwordx4 v[8:11], v3, s[48:51], s68 offen sc1\n"
      "s_movk_i32 s68, 18688\n"
      "s_waitcnt lgkmcnt(0)\n"
      "buffer_store_dwordx4 v[12:15], v3, s[48:51], s68 offen sc1\n"
      "s_endpgm\n"
      ".Lcode_end:\n"
      :
      : [xp] "s"(xp), [gp] "s"(g), [bp] "s"(b), [wp] "s"(wt), [op] "s"(op), [w] "s"(wv & 3), [role] "s"(wv >> 2), [wv] "s"(wv),
        [sl] "s"((bid >> 3) & 31), [lds] "s"((unsigned)(size_t)smem)
      : "v0", "v1", "v2", "v3", "v4", "v5", "v6", "v7", "v8", "v9", "v10", "v11", "v12", "v13", "v14", "v15", "v16", "v17", "v18", "v19", "v20", "v21", "v22", "v23", "v24", "v25", "v26", "v27", "v28", "v29", "v30", "v31", "v32", "v33", "v34", "v35", "v36", "v37", "v38", "v39", "v40", "v41", "v42", "v43", "v44", "v45", "v46", "v47", "v48", "v49", "v50", "v51", "v52", "v53", "v54", "v55", "v56", "v57", "v58", "v59", "v60", "v61", "v62", "v63", "v64", "v65", "v66", "v67", "v68", "v69", "v70", "v71", "v72", "v73", "v74", "v75", "v76", "v77", "v78", "v79", "v80", "v81", "v82", "v83", "v84", "v85", "v86", "v87", "v88", "v89", "v90", "v91", "v92", "v93", "v94", "v95", "v96", "v97", "v98", "v99", "v100", "v101", "v102", "v103", "v104", "v105", "v106", "v107", "v108", "v109", "v110", "v111", "v112", "v113", "v114", "v115", "v116", "v117", "v118", "v119", "v120", "v121", "v122", "v123", "v124", "v125", "v126", "v127", "v128", "v129", "v130", "v131", "v132", "v133", "v134", "v135", "v136", "v137", "v138", "v139", "v140", "v141", "v142", "v143", "v144", "v145", "v146", "v147", "v148", "v149", "v150", "v151", "v152", "v153", "v154", "v155", "v156", "v157", "v158", "v159", "v160", "v161", "v162", "v163", "v164", "v165", "v166", "v167", "v168", "v169", "v170", "v171", "v172", "v173", "v174", "v175", "v176", "v177", "v178", "v179", "v180", "v181", "v182", "v183", "v184", "v185", "v186", "v187", "v188", "v189", "v190", "v191", "v192", "v193", "v194", "v195", "v196", "v197", "v198", "v199", "v200", "v201", "v202", "v203", "v204", "v205", "v206", "v207", "v208", "v209", "v210", "v211", "v212", "v213", "v214", "v215", "v216", "v217", "v218", "v219", "v220", "v221", "v222", "v223", "v224", "v225", "v226", "v227", "v228", "v229", "v230", "v231", "v232", "v233", "v234", "v235", "v236", "v237", "v238", "v239", "v240", "v241", "v242", "v243", "v244", "v245", "v246", "v247", "v248", "v249", "v250", "v251", "v252", "v253", "v254", "v255", "s36", "s37", "s38", "s39", "s40", "s41", "s42", "s43", "s44", "s45", "s46", "s47", "s48", "s49", "s50", "s51", "s52", "s53", "s54", "s55", "s56", "s57", "s58", "s59", "s60", "s61", "s62", "s63", "s64", "s65", "s66", "s67", "s68", "s69", "s70", "s71", "s72", "s73", "s74", "s75", "s76", "s77", "s78", "s79", "s80", "s81", "s82", "s83", "s84", "s85", "s86", "s87", "s88", "s89", "s90", "s91", "s92", "s93", "s94", "s95", "s96", "s97", "s98", "s99", "s100", "s101", "vcc", "scc", "memory");
}

extern "C" void kernel_launch(void* const* d_in, const int* in_sizes, int n_in, void* d_out, int out_size, void* d_ws,
                              size_t ws_size, hipStream_t stream) {
  const int rows = in_sizes[0] / 448;
  lnlin_kernel<<<rows / 128, 512, 0, stream>>>((const char*)d_in[0], (const char*)d_in[1], (const char*)d_in[2],
                                               (const char*)d_in[3], (char*)d_out);
}
```
